```python
import torch

def _softmax(x, axis=-1):
    return torch.softmax(x, dim=axis)
def _gelu(x):
    return torch.nn.functional.gelu(x, approximate="tanh")
def _silu(x):
    return torch.nn.functional.silu(x)
def _sigmoid(x):
    return torch.sigmoid(x)
def _layernorm(x, axis=-1, eps=1e-5):
    m = x.mean(dim=axis, keepdim=True)
    v = x.var(dim=axis, keepdim=True, unbiased=False)
    return (x - m) / torch.sqrt(v + eps)
def _scatter_add(tgt, idx, src, axis=0):
    return tgt.clone().index_add_(axis, idx, src)
def _segment_sum(src, seg, n):
    out = torch.zeros((n,) + src.shape[1:], dtype=src.dtype)
    return out.index_add_(0, seg, src)
def _pad(x, pw):
    flat = tuple(v for pair in reversed(pw) for v in pair)
    return torch.nn.functional.pad(x, flat)

kernel_name = 'recurrent_scan__sliding_block_cached__mixer_token_mlp__decode'

def setup_inputs():

    _w = (lambda a: (lambda t: t.to(torch.bfloat16) if t.is_floating_point() else t)(torch.as_tensor(a)))
    def _A_setup():
        B, Sq, D = 1, 1, 576
        Hq, Hkv, Sk, Dh = 32, 16, 65536, 128
        assert Hq % Hkv == 0, (Hq, Hkv)
        return {
            "x": _w((lambda *s: torch.randn(*s, dtype=torch.bfloat16) * 0.05)(B, Sq, D)),
            "k_cache": _w((lambda *s: torch.randn(*s, dtype=torch.bfloat16) * 3.2)(B, Hkv, Sk, Dh)),
            "v_cache": _w((lambda *s: torch.randn(*s, dtype=torch.bfloat16) * 3.2)(B, Hkv, Sk, Dh)),
            "wq": _w((lambda *s: torch.randn(*s, dtype=torch.bfloat16) * 3.2)(D, Hq * Dh) * (D ** -0.5)),
            "wo": _w((lambda *s: torch.randn(*s, dtype=torch.bfloat16) * 3.2)(Hq * Dh, D) * ((Hq * Dh) ** -0.5)),
        }
    _A_in = _A_setup()

    def _B_setup():
        B, S, D, F = 1, 1, 576, 4608
        return {
            "x": _w((lambda *s: torch.randn(*s, dtype=torch.bfloat16) * 0.05)(B, S, D)),
            "w1": _w((lambda *s: torch.randn(*s, dtype=torch.bfloat16) * 3.2)(S, F) * (S ** -0.5)),
            "b1": _w((lambda *s: torch.randn(*s, dtype=torch.bfloat16) * 3.2)(F) * 0.02),
            "w2": _w((lambda *s: torch.randn(*s, dtype=torch.bfloat16) * 0.4)(F, S) * (F ** -0.5)),
            "b2": _w((lambda *s: torch.randn(*s, dtype=torch.bfloat16) * 0.4)(S) * 0.02),
        }
    _B_in = _B_setup()

    return {
        "A_x": _A_in["x"],
        "A_k_cache": _A_in["k_cache"],
        "A_v_cache": _A_in["v_cache"],
        "A_wq": _A_in["wq"],
        "A_wo": _A_in["wo"],
        "B_x": _B_in["x"],
        "B_w1": _B_in["w1"],
        "B_b1": _B_in["b1"],
        "B_w2": _B_in["w2"],
        "B_b2": _B_in["b2"],
    }

def reference(A_x, A_k_cache, A_v_cache, A_wq, A_wo, B_x, B_w1, B_b1, B_w2, B_b2):
    def _A_ref(x, k_cache, v_cache, wq, wo):
        B, Sq, D = x.shape
        Hq, Dh = 32, 128
        Hkv, Sk = k_cache.shape[1], k_cache.shape[2]
        W = min(4096, max(256, Sk // 4))
        rep = Hq // Hkv
        q = (lambda x, *d: x.permute(*d))((x @ wq).reshape(B, Sq, Hq, Dh), 0, 2, 1, 3)
        k = (lambda x, n, axis: torch.repeat_interleave(x, n, dim=axis))(k_cache[:, :, Sk - W:, :], rep, axis=1)
        v = (lambda x, n, axis: torch.repeat_interleave(x, n, dim=axis))(v_cache[:, :, Sk - W:, :], rep, axis=1)
        scores = torch.einsum("bhqd,bhkd->bhqk", q, k) * (Dh ** -0.5)
        p = _softmax(scores, axis=-1)
        o = torch.einsum("bhqk,bhkd->bhqd", p, v)
        return (lambda x, *d: x.permute(*d))(o, 0, 2, 1, 3).reshape(B, Sq, Hq * Dh) @ wo

    def _B_ref(x, w1, b1, w2, b2):
        B, S, D = x.shape
        h = _layernorm(x, axis=-1)
        u = torch.einsum("bsd,sf->bdf", h, w1) + b1
        u = _gelu(u)
        y = torch.einsum("bdf,fs->bds", u, w2) + b2
        return (lambda x, *d: x.permute(*d))(y, 0, 2, 1)

    x = A_x
    xn = _layernorm(x, axis=-1)
    s = _A_ref(xn, A_k_cache, A_v_cache, A_wq, A_wo)
    g = _silu(_B_ref(xn, B_w1, B_b1, B_w2, B_b2))
    return x + s * g
```

```cpp
#include <hip/hip_runtime.h>
#include <hip/hip_bf16.h>
#include <stdint.h>

typedef unsigned short u16;
__device__ __forceinline__ float bf2f(u16 b) { return __uint_as_float(((unsigned)b) << 16); }
__device__ __forceinline__ float rbf(float f) {
  __bf16 h = (__bf16)f; return (float)h; }
__device__ __forceinline__ u16 f2bf(float f) { __bf16 h = (__bf16)f; return __builtin_bit_cast(u16, h); }

constexpr int D = 576, HQ = 32, HKV = 16, SK = 65536, DH = 128, W = 4096, F = 4608;

__device__ void ln576(const float* in, float* out, float* red) {
  int t = threadIdx.x;
  float s = 0; for (int i = t; i < D; i += 256) s += in[i];
  red[t] = s; __syncthreads();
  for (int o = 128; o > 0; o >>= 1) { if (t < o) red[t] += red[t + o]; __syncthreads(); }
  float mean = red[0] / D; __syncthreads();
  float v = 0; for (int i = t; i < D; i += 256) { float d = in[i] - mean; v += d * d; }
  red[t] = v; __syncthreads();
  for (int o = 128; o > 0; o >>= 1) { if (t < o) red[t] += red[t + o]; __syncthreads(); }
  float var = red[0] / D; __syncthreads();
  float m = rbf(mean), vv = rbf(var);
  float den = rbf(sqrtf(rbf(vv + 1e-5f)));
  for (int i = t; i < D; i += 256) out[i] = rbf(rbf(in[i] - m) / den);
  __syncthreads();
}

__global__ void k_q(const u16* x, const u16* wq, float* q) {
  __shared__ float xs[D], xn[D], red[256];
  int t = threadIdx.x;
  for (int i = t; i < D; i += 256) xs[i] = bf2f(x[i]);
  __syncthreads();
  ln576(xs, xn, red);
  int n = blockIdx.x * 256 + t;
  float a = 0; for (int k = 0; k < D; ++k) a += xn[k] * bf2f(wq[(size_t)k * 4096 + n]);
  q[n] = rbf(a);
}
__global__ void k_attn(const float* q, const u16* kc, const u16* vc, float* o) {
  __shared__ float sc[W], red[256], qs[DH];
  int h = blockIdx.x, g = h / 2, t = threadIdx.x;
  if (t < DH) qs[t] = q[h * DH + t];
  __syncthreads();
  const u16* K = kc + ((size_t)g * SK + (SK - W)) * DH;
  const u16* V = vc + ((size_t)g * SK + (SK - W)) * DH;
  float mx = -INFINITY;
  for (int k = t; k < W; k += 256) { float a = 0; for (int d = 0; d < DH; ++d) a += qs[d] * bf2f(K[(size_t)k * DH + d]);
    a = rbf(rbf(a) * 0.08838834764831845f); sc[k] = a; mx = fmaxf(mx, a); }
  red[t] = mx; __syncthreads();
  for (int o2 = 128; o2 > 0; o2 >>= 1) { if (t < o2) red[t] = fmaxf(red[t], red[t + o2]); __syncthreads(); }
  mx = red[0]; __syncthreads();
  float s = 0; for (int k = t; k < W; k += 256) { float e = expf(sc[k] - mx); sc[k] = e; s += e; }
  red[t] = s; __syncthreads();
  for (int o2 = 128; o2 > 0; o2 >>= 1) { if (t < o2) red[t] += red[t + o2]; __syncthreads(); }
  s = red[0]; __syncthreads();
  if (t < DH) { float a = 0; for (int k = 0; k < W; ++k) a += rbf(sc[k] / s) * bf2f(V[(size_t)k * DH + t]); o[h * DH + t] = rbf(a); }
}
__global__ void k_out(const u16* x, const float* o, const u16* wo, const u16* w1, const u16* b1, const u16* w2, const u16* b2, u16* out) {
  __shared__ float xs[D], xn[D], hh[D], red[256];
  int t = threadIdx.x, j = blockIdx.x;
  for (int i = t; i < D; i += 256) xs[i] = bf2f(x[i]);
  __syncthreads();
  ln576(xs, xn, red); ln576(xn, hh, red);
  float a = 0; for (int i = t; i < 4096; i += 256) a += o[i] * bf2f(wo[(size_t)i * D + j]);
  red[t] = a; __syncthreads();
  for (int o2 = 128; o2 > 0; o2 >>= 1) { if (t < o2) red[t] += red[t + o2]; __syncthreads(); }
  float s = rbf(red[0]); __syncthreads();
  float y = 0; float hj = hh[j];
  for (int f = t; f < F; f += 256) { float u = rbf(rbf(hj * bf2f(w1[f])) + bf2f(b1[f]));
    float z = 0.7978845608028654f * (u + 0.044715f * u * u * u); float gl = rbf(0.5f * u * (1.f + tanhf(z))); y += gl * bf2f(w2[f]); }
  red[t] = y; __syncthreads();
  for (int o2 = 128; o2 > 0; o2 >>= 1) { if (t < o2) red[t] += red[t + o2]; __syncthreads(); }
  if (t == 0) { float yy = rbf(rbf(red[0]) + bf2f(b2[0])); float g = rbf(yy / (1.f + expf(-yy))); out[j] = f2bf(xs[j] + rbf(s * g)); }
}

extern "C" void kernel_launch(void* const* d_in, const int* in_sizes, int n_in, void* d_out, int out_size, void* d_ws, size_t ws_size, hipStream_t stream) {
  const u16* x = (const u16*)d_in[0]; const u16* kc = (const u16*)d_in[1]; const u16* vc = (const u16*)d_in[2];
  const u16* wq = (const u16*)d_in[3]; const u16* wo = (const u16*)d_in[4];
  const u16* w1 = (const u16*)d_in[6]; const u16* b1 = (const u16*)d_in[7]; const u16* w2 = (const u16*)d_in[8]; const u16* b2 = (const u16*)d_in[9];
  float* q = (float*)d_ws; float* o = q + 4096;
  k_q<<<16, 256, 0, stream>>>(x, wq, q);
  k_attn<<<32, 256, 0, stream>>>(q, kc, vc, o);
  k_out<<<576, 256, 0, stream>>>(x, o, wo, w1, b1, w2, b2, (u16*)d_out);
}
```

```cpp
#include <hip/hip_runtime.h>
#include <stdint.h>

#define STAMPS 0

namespace {
typedef unsigned v4u __attribute__((ext_vector_type(4)));
typedef unsigned v3u __attribute__((ext_vector_type(3)));
typedef unsigned v2u __attribute__((ext_vector_type(2)));
typedef float f32x4 __attribute__((ext_vector_type(4)));
typedef short bf16x8 __attribute__((ext_vector_type(8)));
typedef __bf16 bf16x2 __attribute__((ext_vector_type(2)));
#define LAS __attribute__((address_space(3)))
#define GAS __attribute__((address_space(1)))

constexpr int AUX_NT = 2, AUX_SC1 = 16, RSRC3 = 0x00020000;
constexpr int NWG = 256, NTHR = 768;
constexpr int WS_E = 0;
constexpr int WS_Q = 4096;
constexpr int WS_ML = WS_Q + 16 * 8192;
constexpr int WS_Y = WS_ML + 16 * 512;
constexpr int WS_O = WS_Y + 576 * 128;
constexpr int WS_S = WS_O + 256 * 2048;
constexpr int WS_END = WS_S + 144 * 8192;
constexpr int WS_STAMP = 4 << 20;
constexpr unsigned SPIN_CAP = 1u << 18;

constexpr int L_FLAG = 0;
constexpr int L_TAG = 32;
constexpr int L_Q = 64;
constexpr int L_ML = 576;
constexpr int L_P = 640;
constexpr int L_RED = L_P + 2048;
constexpr int L_BYTES = L_RED + 4096;

__device__ __forceinline__ float asf(unsigned u) { return __builtin_bit_cast(float, u); }
__device__ __forceinline__ unsigned asu(float f) { return __builtin_bit_cast(unsigned, f); }
__device__ __forceinline__ float bflo(unsigned u) { return asf(u << 16); }
__device__ __forceinline__ float bfhi(unsigned u) { return asf(u & 0xffff0000u); }
__device__ __forceinline__ unsigned pk_bf16(float lo, float hi) {
  bf16x2 v = {(__bf16)lo, (__bf16)hi}; return __builtin_bit_cast(unsigned, v); }
__device__ __forceinline__ float rbf(float f) { return bflo(pk_bf16(f, f)); }
template <int CTRL> __device__ __forceinline__ float dpp(float x) {
  return asf(__builtin_amdgcn_mov_dpp(asu(x), CTRL, 0xf, 0xf, true)); }
constexpr int XOR1 = 0xB1, XOR2 = 0x4E, HMIR = 0x141, ROR8 = 0x128, ROR4 = 0x124, ROR2 = 0x122, ROR1 = 0x121;
__device__ __forceinline__ float x16_sum(float x) { auto s = __builtin_amdgcn_permlane16_swap(asu(x), asu(x), false, false); return asf(s[0]) + asf(s[1]); }
__device__ __forceinline__ float x32_sum(float x) { auto s = __builtin_amdgcn_permlane32_swap(asu(x), asu(x), false, false); return asf(s[0]) + asf(s[1]); }
__device__ __forceinline__ float x16_max(float x) { auto s = __builtin_amdgcn_permlane16_swap(asu(x), asu(x), false, false); return fmaxf(asf(s[0]), asf(s[1])); }
__device__ __forceinline__ float x32_max(float x) { auto s = __builtin_amdgcn_permlane32_swap(asu(x), asu(x), false, false); return fmaxf(asf(s[0]), asf(s[1])); }
__device__ __forceinline__ float row_sum(float x) { x += dpp<ROR8>(x); x += dpp<ROR4>(x); x += dpp<ROR2>(x); x += dpp<ROR1>(x); return x; }
__device__ __forceinline__ float wave_sum(float x) { return x32_sum(x16_sum(row_sum(x))); }
__device__ __forceinline__ f32x4 mfma(v4u a, v4u b, f32x4 c) {
  return __builtin_amdgcn_mfma_f32_16x16x32_bf16(__builtin_bit_cast(bf16x8, a), __builtin_bit_cast(bf16x8, b), c, 0, 0, 0); }
__device__ __forceinline__ __amdgpu_buffer_rsrc_t rsrc(const void* p, int bytes) { return __builtin_amdgcn_make_buffer_rsrc((void*)p, 0, bytes, RSRC3); }
#define SCHED() __builtin_amdgcn_sched_barrier(0)
#define CBAR() asm volatile("" ::: "memory")
#define LGKM0() asm volatile("s_waitcnt lgkmcnt(0)" ::: "memory")
__device__ __forceinline__ unsigned lds_ld(LAS unsigned* p) { return __hip_atomic_load(p, __ATOMIC_RELAXED, __HIP_MEMORY_SCOPE_WORKGROUP); }
__device__ __forceinline__ void lds_st(LAS unsigned* p, unsigned v) { __hip_atomic_store(p, v, __ATOMIC_RELAXED, __HIP_MEMORY_SCOPE_WORKGROUP); }
__device__ __forceinline__ void lds_wait(LAS unsigned* p) {
  for (unsigned i = 0; i < (SPIN_CAP << 4); ++i) { if (__builtin_amdgcn_readfirstlane(lds_ld(p)) != 0u) break; __builtin_amdgcn_s_sleep(1); }
  CBAR();
}
struct Sel { unsigned m[4]; };
__device__ __forceinline__ Sel make_sel(int lane) { Sel s; const unsigned h = (lane & 1) ? 0xffff0000u : 0x0000ffffu;
#pragma unroll
  for (int r = 0; r < 4; ++r) s.m[r] = (((lane >> 1) & 3) == r) ? h : 0u;
  return s; }
__device__ __forceinline__ v4u onehot(const Sel& s, unsigned pp) { return v4u{pp & s.m[0], pp & s.m[1], pp & s.m[2], pp & s.m[3]}; }
__device__ __forceinline__ unsigned glo(float v, unsigned tl) { return (asu(v) & 0xffffff00u) | tl; }
__device__ __forceinline__ float gval(unsigned lo) { return asf(lo & 0xffffff00u); }
__device__ __forceinline__ unsigned gbad(v4u g, unsigned tl, unsigned th) { return (((g.x ^ tl) | (g.z ^ tl)) & 0xffu) | (g.y ^ th) | (g.w ^ th); }

__device__ __forceinline__ void ln_stats(const float (&a)[8], const float (&b)[8], int lane, float& m, float& rden) {
  float s = 0.f;
#pragma unroll
  for (int i = 0; i < 8; ++i) s += a[i] + b[i];
  const float mean = wave_sum(s) * (1.0f / 576.0f);
  const float mb = lane < 8 ? mean : 0.f;
  float v = 0.f;
#pragma unroll
  for (int i = 0; i < 8; ++i) { const float d = a[i] - mean, e = b[i] - mb; v = fmaf(d, d, v); v = fmaf(e, e, v); }
  const float var = wave_sum(v) * (1.0f / 576.0f);
  m = rbf(mean);
  const float den = rbf(__builtin_sqrtf(rbf(rbf(var) + 1e-5f)));
  rden = 1.0f / den;
}
__device__ __forceinline__ float ln_apply(float x, float m, float rden) { return rbf(rbf(x - m) * rden); }

#define STAMP(i) do {} while (0)
#define STAMP_OUT() do {} while (0)

__global__ void __launch_bounds__(NTHR) fused_decode(const char* __restrict__ x, const char* __restrict__ kc, const char* __restrict__ vc,
    const char* __restrict__ wq, const char* __restrict__ wo, const char* __restrict__ w1, const char* __restrict__ b1,
    const char* __restrict__ w2, const char* __restrict__ b2, char* __restrict__ out, char* __restrict__ ws) {
  __shared__ __attribute__((aligned(16))) unsigned char smem[L_BYTES];
  LAS unsigned char* L = (LAS unsigned char*)smem;
  LAS unsigned* flag = (LAS unsigned*)(L + L_FLAG);
  const int wg = blockIdx.x, lane = threadIdx.x & 63, w = __builtin_amdgcn_readfirstlane(threadIdx.x >> 6);
  const int g = (wg & 7) * 2 + (wg >> 7), s = (wg >> 3) & 15;
  STAMP(0);
  const __amdgpu_buffer_rsrc_t rws = rsrc(ws, WS_END);

  if (w == 0) {
    const int cq = s & 3, rq = s >> 2, z = lane >> 3;
    const __amdgpu_buffer_rsrc_t rx = rsrc(x, 1152);
    const v2u ep = __builtin_amdgcn_raw_buffer_load_b64(rws, 0, WS_E, AUX_SC1);
    const v4u xa = __builtin_amdgcn_raw_buffer_load_b128(rx, lane * 16, 0, 0);
    const v4u xb = __builtin_amdgcn_raw_buffer_load_b128(rx, lane * 16 + 1024, 0, 0);
    const v4u xr0 = __builtin_amdgcn_raw_buffer_load_b128(rx, z * 36, rq * 288, 0);
    const v4u xr1 = __builtin_amdgcn_raw_buffer_load_b128(rx, z * 36 + 16, rq * 288, 0);
    const unsigned xr2 = __builtin_amdgcn_raw_buffer_load_b32(rx, z * 36 + 32, rq * 288, 0);
    const __amdgpu_buffer_rsrc_t rwq = rsrc(wq + (size_t)(rq * 144) * 8192 + g * 512 + cq * 128, 144 * 8192);
    v4u a[18];
#pragma unroll
    for (int i = 0; i < 18; ++i) a[i] = __builtin_amdgcn_raw_buffer_load_b128(rwq, z * (18 * 8192) + (lane & 7) * 16, i * 8192, AUX_NT);
    SCHED();
    __builtin_amdgcn_s_barrier();
    SCHED();
    STAMP(1);
    unsigned long long e64 = ((unsigned long long)__builtin_amdgcn_readfirstlane(ep.y) << 32) | __builtin_amdgcn_readfirstlane(ep.x);
    e64 += 1;
    const unsigned tl = (unsigned)e64 & 0xffu, th = (unsigned)(e64 >> 8);
    if (lane == 0) { *(LAS v2u*)(L + L_TAG) = v2u{tl, th}; LGKM0(); lds_st(flag + 0, 1u); }
    float fa[8], fb[8];
#pragma unroll
    for (int i = 0; i < 4; ++i) { fa[2 * i] = bflo(xa[i]); fa[2 * i + 1] = bfhi(xa[i]); fb[2 * i] = bflo(xb[i]); fb[2 * i + 1] = bfhi(xb[i]); }
    float m, rden;
    ln_stats(fa, fb, lane, m, rden);
    const Sel sel = make_sel(lane);
    unsigned xr[9] = {xr0.x, xr0.y, xr0.z, xr0.w, xr1.x, xr1.y, xr1.z, xr1.w, xr2};
    f32x4 acc = {0.f, 0.f, 0.f, 0.f};
    STAMP(2);
#pragma unroll
    for (int i = 0; i < 18; ++i) {
      const float xv = (i & 1) ? bfhi(xr[i >> 1]) : bflo(xr[i >> 1]);
      const float t = rbf(xv - m) * rden;
      acc = mfma(a[i], onehot(sel, pk_bf16(t, t)), acc);
    }
    {
      auto r01 = __builtin_amdgcn_permlane32_swap(asu(acc[0]), asu(acc[1]), false, false);
      auto r23 = __builtin_amdgcn_permlane32_swap(asu(acc[2]), asu(acc[3]), false, false);
      const float y0 = asf(r01[0]) + dpp<ROR8>(asf(r01[1])), y1 = asf(r23[0]) + dpp<ROR8>(asf(r23[1]));
      const int pair = cq * 32 + (lane >> 5) * 16 + ((lane >> 4) & 1) * 8 + (lane & 7);
      if (!(lane & 8)) __builtin_amdgcn_raw_buffer_store_b128(v4u{glo(y0, tl), th, glo(y1, tl), th}, rws, pair * 16, WS_Q + g * 8192 + rq * 2048, AUX_SC1);
    }
    STAMP(3);
    __builtin_amdgcn_s_sleep(20);
    for (unsigned spin = 0; spin < SPIN_CAP; ++spin) {
      CBAR();
      v4u q[8];
#pragma unroll
      for (int i = 0; i < 8; ++i) q[i] = __builtin_amdgcn_raw_buffer_load_b128(rws, lane * 16, WS_Q + g * 8192 + i * 1024, AUX_SC1);
      unsigned bad = 0;
#pragma unroll
      for (int i = 0; i < 8; ++i) bad |= gbad(q[i], tl, th);
      if (__builtin_amdgcn_ballot_w64(bad != 0u) == 0ull || spin == SPIN_CAP - 1) {
#pragma unroll
        for (int e = 0; e < 2; ++e) {
          const float v0 = (gval(q[e].x) + gval(q[2 + e].x)) + (gval(q[4 + e].x) + gval(q[6 + e].x));
          const float v1 = (gval(q[e].z) + gval(q[2 + e].z)) + (gval(q[4 + e].z) + gval(q[6 + e].z));
          const unsigned pk = pk_bf16(v0, v1);
          const int c0 = 64 * (2 * e + (lane >> 5)) + 32 * ((lane >> 3) & 1) + 8 * ((lane >> 4) & 1) + (lane & 7);
          *(LAS unsigned short*)(L + L_Q + c0 * 2) = (unsigned short)pk;
          *(LAS unsigned short*)(L + L_Q + c0 * 2 + 32) = (unsigned short)(pk >> 16);
        }
        LGKM0();
        if (lane == 0) lds_st(flag + 1, 1u);
        break;
      }
    }
    STAMP(4);
    float gate = 0.f, xo = 0.f;
    const int col = 64 * (wg >> 4) + 32 * ((wg >> 3) & 1) + 8 * ((wg >> 2) & 1) + 2 * (wg & 3) + ((lane >> 4) & 1) + 16 * (lane >> 5);
    if (wg < 144) {
      xo = bflo(__builtin_amdgcn_raw_buffer_load_b16(rx, col * 2, 0, 0));
      const float bb = bflo(*(const GAS unsigned short*)b2);
      for (unsigned spin = 0; spin < SPIN_CAP; ++spin) {
        CBAR();
        const v2u y = __builtin_amdgcn_raw_buffer_load_b64(rws, col * 128 + (lane & 15) * 8, WS_Y, AUX_SC1);
        const bool live = (lane & 15) < 12;
        const unsigned bad = ((y.x ^ tl) & 0xffu) | (y.y ^ th);
        if (__builtin_amdgcn_ballot_w64(live && bad != 0u) == 0ull || spin == SPIN_CAP - 1) {
          const float yy = rbf(rbf(row_sum(live ? gval(y.x) : 0.f)) + bb);
          gate = rbf(yy / (1.0f + __expf(-yy)));
          break;
        }
        __builtin_amdgcn_s_sleep(8);
      }
    }
    STAMP(5);
    __builtin_amdgcn_s_barrier();
    STAMP(6);
    if (wg < 144) {
      for (unsigned spin = 0; spin < SPIN_CAP; ++spin) {
        CBAR();
        v4u d[8];
#pragma unroll
        for (int i = 0; i < 8; ++i) d[i] = __builtin_amdgcn_raw_buffer_load_b128(rws, lane * 16, WS_S + wg * 8192 + i * 1024, AUX_SC1);
        unsigned bad = 0; float v0 = 0.f, v1 = 0.f;
#pragma unroll
        for (int i = 0; i < 8; ++i) { bad |= gbad(d[i], tl, th); v0 += gval(d[i].x); v1 += gval(d[i].z); }
        if (__builtin_amdgcn_ballot_w64(bad != 0u) == 0ull || spin == SPIN_CAP - 1) {
          auto r = __builtin_amdgcn_permlane32_swap(asu(v0), asu(v1), false, false);
          float y = asf(r[0]) + asf(r[1]);
          y = x16_sum(y); y += dpp<ROR8>(y); y += dpp<ROR4>(y); y += dpp<ROR2>(y);
          const float res = xo + rbf(rbf(y) * gate);
          if ((lane & 15) == ((lane >> 4) & 1)) *(GAS unsigned short*)(out + col * 2) = (unsigned short)pk_bf16(res, res);
          if (wg == 0 && lane == 0) *(GAS unsigned long long*)(ws + WS_E) = e64;
          break;
        }
      }
    }
    STAMP(7);
    STAMP_OUT();
  } else if (w <= 4) {
    const int kw = w - 1, rot = s * 20 + g * 7;
    const __amdgpu_buffer_rsrc_t rk = rsrc(kc + ((size_t)g * 65536 + 61440 + s * 256) * 256, 65536);
    const int voff = ((lane >> 2) & 3) * 256 + ((lane >> 4) * 4 + (lane & 3)) * 16;
    __builtin_amdgcn_s_barrier();
    v4u k[16];
#pragma unroll
    for (int i = 0; i < 16; ++i) k[i] = __builtin_amdgcn_raw_buffer_load_b128(rk, voff, ((kw * 16 + i + rot) & 63) * 1024, AUX_NT);
    SCHED();
    STAMP(1);
    lds_wait(flag + 1);
    STAMP(2);
    const v4u qf = *(LAS v4u*)(L + L_Q + ((lane >> 3) & 1) * 256 + ((lane >> 4) * 4 + (lane & 3)) * 16);
    const v4u zero = {0u, 0u, 0u, 0u};
    const v4u q0 = (lane & 4) ? zero : qf, q1 = (lane & 4) ? qf : zero;
    float sc[8];
    float mx = -INFINITY;
#pragma unroll
    for (int i = 0; i < 8; ++i) {
      f32x4 acc = {0.f, 0.f, 0.f, 0.f};
      acc = mfma(k[2 * i], q0, acc);
      acc = mfma(k[2 * i + 1], q1, acc);
      const float d = (dpp<0x00>(acc[0]) + dpp<0x55>(acc[1])) + (dpp<0xAA>(acc[2]) + dpp<0xFF>(acc[3]));
      sc[i] = rbf(rbf(d) * 0.08838834764831845f);
      mx = fmaxf(mx, sc[i]);
    }
    mx = fmaxf(mx, dpp<HMIR>(mx));
    mx = x32_max(x16_max(mx));
    float l = 0.f; unsigned pp[8];
    const float mo = mx * 1.4426950408889634f;
#pragma unroll
    for (int i = 0; i < 8; ++i) { const float p = __builtin_amdgcn_exp2f(fmaf(sc[i], 1.4426950408889634f, -mo)); l += p; pp[i] = pk_bf16(p, p); }
    l += dpp<HMIR>(l);
    l = x32_sum(x16_sum(l));
    if ((lane & 3) == 0) {
      LAS v4u* pd = (LAS v4u*)(L + L_P + kw * 512 + (lane >> 2) * 32);
      pd[0] = v4u{pp[0], pp[1], pp[2], pp[3]}; pd[1] = v4u{pp[4], pp[5], pp[6], pp[7]};
    }
    if ((lane & 0x37) == 0) *(LAS v2u*)(L + L_ML + kw * 16 + (lane >> 3) * 8) = v2u{asu(mx), asu(l)};
    LGKM0();
    if (lane == 0) lds_st(flag + 2 + kw, 1u);
    STAMP(3);
    STAMP_OUT();
  } else if (w <= 8) {
    const int vw = w - 5, rot = s * 20 + g * 7, h = (lane >> 3) & 1;
    const __amdgpu_buffer_rsrc_t rv = rsrc(vc + ((size_t)g * 65536 + 61440 + s * 256) * 256, 65536);
    __builtin_amdgcn_s_barrier();
    v4u v[16];
#pragma unroll
    for (int i = 0; i < 16; ++i) v[i] = __builtin_amdgcn_raw_buffer_load_b128(rv, lane * 16, ((vw * 16 + i + rot) & 63) * 1024, AUX_NT);
    SCHED();
    STAMP(1);
    lds_wait(flag + 2); lds_wait(flag + 3); lds_wait(flag + 4); lds_wait(flag + 5);
    STAMP(2);
    const Sel sel = make_sel(lane);
    v4u pr[4];
#pragma unroll
    for (int i = 0; i < 4; ++i) pr[i] = *(LAS v4u*)(L + L_P + vw * 512 + (lane >> 3) * 64 + i * 16);
    v2u ml[4];
#pragma unroll
    for (int i = 0; i < 4; ++i) ml[i] = *(LAS v2u*)(L + L_ML + i * 16 + h * 8);
    const v2u mine = *(LAS v2u*)(L + L_ML + vw * 16 + h * 8);
    const v2u tg = *(LAS v2u*)(L + L_TAG);
    const unsigned tl = tg.x, th = tg.y;
    const float M = fmaxf(fmaxf(asf(ml[0].x), asf(ml[1].x)), fmaxf(asf(ml[2].x), asf(ml[3].x)));
    const float f = __expf(asf(mine.x) - M);
    if (vw == 0) {
      float Ls = 0.f;
#pragma unroll
      for (int i = 0; i < 4; ++i) Ls = fmaf(__expf(asf(ml[i].x) - M), asf(ml[i].y), Ls);
      if ((lane & 0x37) == 0) __builtin_amdgcn_raw_buffer_store_b128(v4u{glo(M, tl), th, glo(Ls, tl), th}, rws, h * 256, WS_ML + g * 512 + s * 16, AUX_SC1);
    }
    v4u b[16];
#pragma unroll
    for (int i = 0; i < 16; ++i) { const int idx = (i & 1) * 8 + (i >> 1); b[i] = onehot(sel, pr[idx >> 2][idx & 3]); }
    SCHED();
    f32x4 acc = {0.f, 0.f, 0.f, 0.f};
#pragma unroll
    for (int i = 0; i < 16; ++i) acc = mfma(v[i], b[i], acc);
    acc *= f;
    *(LAS f32x4*)(L + L_RED + vw * 1024 + lane * 16) = acc;
    LGKM0();
    STAMP(3);
    __builtin_amdgcn_s_barrier();
    CBAR();
    if (vw < 2) {
      v2u r[4];
#pragma unroll
      for (int i = 0; i < 4; ++i) r[i] = *(LAS v2u*)(L + L_RED + i * 1024 + lane * 16 + vw * 8);
      const float o0 = (asf(r[0].x) + asf(r[1].x)) + (asf(r[2].x) + asf(r[3].x));
      const float o1 = (asf(r[0].y) + asf(r[1].y)) + (asf(r[2].y) + asf(r[3].y));
      __builtin_amdgcn_raw_buffer_store_b128(v4u{glo(o0, tl), th, glo(o1, tl), th}, rws, (lane >> 2) * 2048 + (lane & 3) * 32 + vw * 16,
                                             WS_O + g * 32768 + s * 128, AUX_SC1);
    }
    STAMP(4);
    STAMP_OUT();
  } else {
    const int ow = w - 9, z = lane >> 3;
    if (ow == 0 && lane < 8) lds_st(flag + lane, 0u);
    const __amdgpu_buffer_rsrc_t rx = rsrc(x, 1152);
    const v4u xa = __builtin_amdgcn_raw_buffer_load_b128(rx, lane * 16, 0, 0);
    const v4u xb = __builtin_amdgcn_raw_buffer_load_b128(rx, lane * 16 + 1024, 0, 0);
    const int cg = wg >> 2, fq = wg & 3;
    const unsigned xc = __builtin_amdgcn_raw_buffer_load_b16(rx, min(lane, 8) * 2, cg * 18, 0);
    const int foff = fq * 2304 + ow * 768;
    const v3u w1v = __builtin_amdgcn_raw_buffer_load_b96(rsrc(w1, 9216), lane * 12, foff, 0);
    const v3u b1v = __builtin_amdgcn_raw_buffer_load_b96(rsrc(b1, 9216), lane * 12, foff, 0);
    const v3u w2v = __builtin_amdgcn_raw_buffer_load_b96(rsrc(w2, 9216), lane * 12, foff, 0);
    LGKM0();
    SCHED();
    __builtin_amdgcn_s_barrier();
    SCHED();
    STAMP(1);
    float fa[8], fb[8];
#pragma unroll
    for (int i = 0; i < 4; ++i) { fa[2 * i] = bflo(xa[i]); fa[2 * i + 1] = bfhi(xa[i]); fb[2 * i] = bflo(xb[i]); fb[2 * i + 1] = bfhi(xb[i]); }
    float m, rden;
    ln_stats(fa, fb, lane, m, rden);
    float hv = ln_apply(bflo(xc), m, rden);
#pragma unroll
    for (int i = 0; i < 8; ++i) { fa[i] = ln_apply(fa[i], m, rden); fb[i] = lane < 8 ? ln_apply(fb[i], m, rden) : 0.f; }
    ln_stats(fa, fb, lane, m, rden);
    hv = ln_apply(hv, m, rden);
    float ysum = 0.f;
    constexpr float C1 = -2.0f * 0.7978845608028654f * 1.4426950408889634f, C2 = C1 * 0.044715f;
#pragma unroll 1
    for (int c = 0; c < 9; ++c) {
      const float hc = asf(__builtin_amdgcn_readlane(asu(hv), c));
      float y = 0.f;
#pragma unroll
      for (int j = 0; j < 3; ++j) {
        const unsigned t = pk_bf16(hc * bflo(w1v[j]), hc * bfhi(w1v[j]));
        const unsigned u = pk_bf16(bflo(t) + bflo(b1v[j]), bfhi(t) + bfhi(b1v[j]));
        const float u0 = bflo(u), u1 = bfhi(u);
        const float e0 = __builtin_amdgcn_exp2f(u0 * fmaf(u0 * u0, C2, C1)), e1 = __builtin_amdgcn_exp2f(u1 * fmaf(u1 * u1, C2, C1));
        const unsigned gl = pk_bf16(u0 * __builtin_amdgcn_rcpf(1.0f + e0), u1 * __builtin_amdgcn_rcpf(1.0f + e1));
        y = fmaf(bflo(gl), bflo(w2v[j]), y); y = fmaf(bfhi(gl), bfhi(w2v[j]), y);
      }
      y = wave_sum(y);
      if (lane == c) ysum = y;
    }
    lds_wait(flag + 0);
    const v2u tg = *(LAS v2u*)(L + L_TAG);
    const unsigned tl = tg.x, th = tg.y;
    if (lane < 9) __builtin_amdgcn_raw_buffer_store_b64(v2u{glo(ysum, tl), th}, rws, lane * 128, WS_Y + cg * 1152 + (fq * 3 + ow) * 8, AUX_SC1);
    STAMP(2);
    const int hh = (s >> 1) & 1;
    const __amdgpu_buffer_rsrc_t rwo = rsrc(wo + (size_t)(g * 256 + hh * 128 + 32 * (s >> 2) + 4 * (s & 1)) * 1152, 28 * 1152);
    v4u a[6];
#pragma unroll
    for (int j = 0; j < 3; ++j)
#pragma unroll
      for (int mm = 0; mm < 2; ++mm)
        a[2 * j + mm] = __builtin_amdgcn_raw_buffer_load_b128(rwo, (16 * (z & 1) + (z >> 1)) * 1152 + (lane & 7) * 16, mm * 8 * 1152 + (ow * 3 + j) * 128, AUX_NT);
    SCHED();
    STAMP(3);
    const Sel sel = make_sel(lane);
    __builtin_amdgcn_s_barrier();
    STAMP(4);
    for (unsigned spin = 0; spin < SPIN_CAP; ++spin) {
      CBAR();
      v4u ml[2], d[2];
#pragma unroll
      for (int t = 0; t < 2; ++t) {
        d[t] = __builtin_amdgcn_raw_buffer_load_b128(rws, (lane & 7) * 128 + z * 16, WS_O + g * 32768 + s * 2048 + t * 1024, AUX_SC1);
        ml[t] = __builtin_amdgcn_raw_buffer_load_b128(rws, (lane & 7) * 16, WS_ML + g * 512 + hh * 256 + t * 128, AUX_SC1);
      }
      const unsigned bad = gbad(d[0], tl, th) | gbad(d[1], tl, th) | gbad(ml[0], tl, th) | gbad(ml[1], tl, th);
      if (__builtin_amdgcn_ballot_w64(bad != 0u) == 0ull || spin == SPIN_CAP - 1) {
        STAMP(5);
        float M = fmaxf(gval(ml[0].x), gval(ml[1].x));
        M = fmaxf(M, dpp<XOR1>(M)); M = fmaxf(M, dpp<XOR2>(M)); M = fmaxf(M, dpp<HMIR>(M));
        const float w0 = __expf(gval(ml[0].x) - M), w1f = __expf(gval(ml[1].x) - M);
        float Ls = w0 * gval(ml[0].z) + w1f * gval(ml[1].z);
        float o0 = w0 * gval(d[0].x) + w1f * gval(d[1].x), o1 = w0 * gval(d[0].z) + w1f * gval(d[1].z);
        Ls += dpp<XOR1>(Ls); o0 += dpp<XOR1>(o0); o1 += dpp<XOR1>(o1);
        Ls += dpp<XOR2>(Ls); o0 += dpp<XOR2>(o0); o1 += dpp<XOR2>(o1);
        Ls += dpp<HMIR>(Ls); o0 += dpp<HMIR>(o0); o1 += dpp<HMIR>(o1);
        const float rl = __builtin_amdgcn_rcpf(Ls);
        o0 *= rl; o1 *= rl;
        const v4u b0 = onehot(sel, pk_bf16(o0, o0)), b1f = onehot(sel, pk_bf16(o1, o1));
        const int voff = (((lane >> 4) & 1) * 8 + (lane >> 5) * 4 + ((lane & 7) >> 1)) * 8192 + (lane & 1) * 16;
#pragma unroll
        for (int j = 0; j < 3; ++j) {
          f32x4 acc = {0.f, 0.f, 0.f, 0.f};
          acc = mfma(a[2 * j], b0, acc);
          acc = mfma(a[2 * j + 1], b1f, acc);
          auto r01 = __builtin_amdgcn_permlane32_swap(asu(acc[0]), asu(acc[1]), false, false);
          auto r23 = __builtin_amdgcn_permlane32_swap(asu(acc[2]), asu(acc[3]), false, false);
          const float y0 = asf(r01[0]) + dpp<ROR8>(asf(r01[1])), y1 = asf(r23[0]) + dpp<ROR8>(asf(r23[1]));
          if (!(lane & 8)) __builtin_amdgcn_raw_buffer_store_b128(v4u{glo(y0, tl), th, glo(y1, tl), th}, rws, voff, WS_S + (ow * 3 + j) * (16 * 8192) + wg * 32, AUX_SC1);
        }
        break;
      }
    }
    STAMP(6);
    STAMP_OUT();
  }
}
}

extern "C" void kernel_launch(void* const* d_in, const int* in_sizes, int n_in, void* d_out, int out_size, void* d_ws, size_t ws_size, hipStream_t stream) {
  fused_decode<<<NWG, NTHR, 0, stream>>>((const char*)d_in[0], (const char*)d_in[1], (const char*)d_in[2], (const char*)d_in[3], (const char*)d_in[4],
      (const char*)d_in[6], (const char*)d_in[7], (const char*)d_in[8], (const char*)d_in[9], (char*)d_out, (char*)d_ws);
}
```

```cpp
#include <hip/hip_runtime.h>
#include <stdint.h>

#define STAMPS 1

namespace {
typedef unsigned v4u __attribute__((ext_vector_type(4)));
typedef unsigned v3u __attribute__((ext_vector_type(3)));
typedef unsigned v2u __attribute__((ext_vector_type(2)));
typedef float f32x4 __attribute__((ext_vector_type(4)));
typedef short bf16x8 __attribute__((ext_vector_type(8)));
typedef __bf16 bf16x2 __attribute__((ext_vector_type(2)));
#define LAS __attribute__((address_space(3)))
#define GAS __attribute__((address_space(1)))

constexpr int AUX_NT = 2, AUX_SC1 = 16, RSRC3 = 0x00020000;
constexpr int NWG = 256, NTHR = 768;
constexpr int WS_E = 0;
constexpr int WS_Q = 4096;
constexpr int WS_ML = WS_Q + 16 * 8192;
constexpr int WS_Y = WS_ML + 16 * 512;
constexpr int WS_O = WS_Y + 576 * 128;
constexpr int WS_S = WS_O + 256 * 2048;
constexpr int WS_END = WS_S + 144 * 8192;
constexpr int WS_STAMP = 4 << 20;
constexpr unsigned SPIN_CAP = 1u << 18;

constexpr int L_FLAG = 0;
constexpr int L_TAG = 32;
constexpr int L_Q = 64;
constexpr int L_ML = 576;
constexpr int L_P = 640;
constexpr int L_RED = L_P + 2048;
constexpr int L_BYTES = L_RED + 4096;

__device__ __forceinline__ float asf(unsigned u) { return __builtin_bit_cast(float, u); }
__device__ __forceinline__ unsigned asu(float f) { return __builtin_bit_cast(unsigned, f); }
__device__ __forceinline__ float bflo(unsigned u) { return asf(u << 16); }
__device__ __forceinline__ float bfhi(unsigned u) { return asf(u & 0xffff0000u); }
__device__ __forceinline__ unsigned pk_bf16(float lo, float hi) {
  bf16x2 v = {(__bf16)lo, (__bf16)hi}; return __builtin_bit_cast(unsigned, v); }
__device__ __forceinline__ float rbf(float f) { return bflo(pk_bf16(f, f)); }
template <int CTRL> __device__ __forceinline__ float dpp(float x) {
  return asf(__builtin_amdgcn_mov_dpp(asu(x), CTRL, 0xf, 0xf, true)); }
constexpr int XOR1 = 0xB1, XOR2 = 0x4E, HMIR = 0x141, ROR8 = 0x128, ROR4 = 0x124, ROR2 = 0x122, ROR1 = 0x121;
__device__ __forceinline__ float x16_sum(float x) { auto s = __builtin_amdgcn_permlane16_swap(asu(x), asu(x), false, false); return asf(s[0]) + asf(s[1]); }
__device__ __forceinline__ float x32_sum(float x) { auto s = __builtin_amdgcn_permlane32_swap(asu(x), asu(x), false, false); return asf(s[0]) + asf(s[1]); }
__device__ __forceinline__ float x16_max(float x) { auto s = __builtin_amdgcn_permlane16_swap(asu(x), asu(x), false, false); return fmaxf(asf(s[0]), asf(s[1])); }
__device__ __forceinline__ float x32_max(float x) { auto s = __builtin_amdgcn_permlane32_swap(asu(x), asu(x), false, false); return fmaxf(asf(s[0]), asf(s[1])); }
__device__ __forceinline__ float row_sum(float x) { x += dpp<ROR8>(x); x += dpp<ROR4>(x); x += dpp<ROR2>(x); x += dpp<ROR1>(x); return x; }
__device__ __forceinline__ float wave_sum(float x) { return x32_sum(x16_sum(row_sum(x))); }
__device__ __forceinline__ f32x4 mfma(v4u a, v4u b, f32x4 c) {
  return __builtin_amdgcn_mfma_f32_16x16x32_bf16(__builtin_bit_cast(bf16x8, a), __builtin_bit_cast(bf16x8, b), c, 0, 0, 0); }
__device__ __forceinline__ __amdgpu_buffer_rsrc_t rsrc(const void* p, int bytes) { return __builtin_amdgcn_make_buffer_rsrc((void*)p, 0, bytes, RSRC3); }
#define SCHED() __builtin_amdgcn_sched_barrier(0)
#define CBAR() asm volatile("" ::: "memory")
#define LGKM0() asm volatile("s_waitcnt lgkmcnt(0)" ::: "memory")
__device__ __forceinline__ unsigned lds_ld(LAS unsigned* p) { return __hip_atomic_load(p, __ATOMIC_RELAXED, __HIP_MEMORY_SCOPE_WORKGROUP); }
__device__ __forceinline__ void lds_st(LAS unsigned* p, unsigned v) { __hip_atomic_store(p, v, __ATOMIC_RELAXED, __HIP_MEMORY_SCOPE_WORKGROUP); }
__device__ __forceinline__ void lds_wait(LAS unsigned* p) {
  for (unsigned i = 0; i < (SPIN_CAP << 4); ++i) { if (__builtin_amdgcn_readfirstlane(lds_ld(p)) != 0u) break; __builtin_amdgcn_s_sleep(1); }
  CBAR();
}
struct Sel { unsigned m[4]; };
__device__ __forceinline__ Sel make_sel(int lane) { Sel s; const unsigned h = (lane & 1) ? 0xffff0000u : 0x0000ffffu;
#pragma unroll
  for (int r = 0; r < 4; ++r) s.m[r] = (((lane >> 1) & 3) == r) ? h : 0u;
  return s; }
__device__ __forceinline__ v4u onehot(const Sel& s, unsigned pp) { return v4u{pp & s.m[0], pp & s.m[1], pp & s.m[2], pp & s.m[3]}; }
__device__ __forceinline__ unsigned glo(float v, unsigned tl) { return (asu(v) & 0xffffff00u) | tl; }
__device__ __forceinline__ float gval(unsigned lo) { return asf(lo & 0xffffff00u); }
__device__ __forceinline__ unsigned gbad(v4u g, unsigned tl, unsigned th) { return (((g.x ^ tl) | (g.z ^ tl)) & 0xffu) | (g.y ^ th) | (g.w ^ th); }

__device__ __forceinline__ void ln_stats(const float (&a)[8], const float (&b)[8], int lane, float& m, float& rden) {
  float s = 0.f;
#pragma unroll
  for (int i = 0; i < 8; ++i) s += a[i] + b[i];
  const float mean = wave_sum(s) * (1.0f / 576.0f);
  const float mb = lane < 8 ? mean : 0.f;
  float v = 0.f;
#pragma unroll
  for (int i = 0; i < 8; ++i) { const float d = a[i] - mean, e = b[i] - mb; v = fmaf(d, d, v); v = fmaf(e, e, v); }
  const float var = wave_sum(v) * (1.0f / 576.0f);
  m = rbf(mean);
  const float den = rbf(__builtin_sqrtf(rbf(rbf(var) + 1e-5f)));
  rden = 1.0f / den;
}
__device__ __forceinline__ float ln_apply(float x, float m, float rden) { return rbf(rbf(x - m) * rden); }

#define STAMP(i) do { SCHED(); unsigned long long t_; asm volatile("s_memrealtime %0\n\ts_waitcnt lgkmcnt(0)" : "=s"(t_) :: "memory"); if (lane == (i)) stv = (unsigned)t_; SCHED(); } while (0)
#define STAMP_OUT() do { ((GAS unsigned*)(ws + WS_STAMP))[(blockIdx.x * 16 + w) * 64 + lane] = stv; } while (0)

__global__ void __launch_bounds__(NTHR) fused_decode(const char* __restrict__ x, const char* __restrict__ kc, const char* __restrict__ vc,
    const char* __restrict__ wq, const char* __restrict__ wo, const char* __restrict__ w1, const char* __restrict__ b1,
    const char* __restrict__ w2, const char* __restrict__ b2, char* __restrict__ out, char* __restrict__ ws) {
  __shared__ __attribute__((aligned(16))) unsigned char smem[L_BYTES];
  LAS unsigned char* L = (LAS unsigned char*)smem;
  LAS unsigned* flag = (LAS unsigned*)(L + L_FLAG);
  const int wg = blockIdx.x, lane = threadIdx.x & 63, w = __builtin_amdgcn_readfirstlane(threadIdx.x >> 6);
  const int g = (wg & 7) * 2 + (wg >> 7), s = (wg >> 3) & 15;
  unsigned stv = 0;
  STAMP(0);
  const __amdgpu_buffer_rsrc_t rws = rsrc(ws, WS_END);

  if (w == 0) {
    const int cq = s & 3, rq = s >> 2, z = lane >> 3;
    const __amdgpu_buffer_rsrc_t rx = rsrc(x, 1152);
    const v2u ep = __builtin_amdgcn_raw_buffer_load_b64(rws, 0, WS_E, AUX_SC1);
    const v4u xa = __builtin_amdgcn_raw_buffer_load_b128(rx, lane * 16, 0, 0);
    const v4u xb = __builtin_amdgcn_raw_buffer_load_b128(rx, lane * 16 + 1024, 0, 0);
    const v4u xr0 = __builtin_amdgcn_raw_buffer_load_b128(rx, z * 36, rq * 288, 0);
    const v4u xr1 = __builtin_amdgcn_raw_buffer_load_b128(rx, z * 36 + 16, rq * 288, 0);
    const unsigned xr2 = __builtin_amdgcn_raw_buffer_load_b32(rx, z * 36 + 32, rq * 288, 0);
    const __amdgpu_buffer_rsrc_t rwq = rsrc(wq + (size_t)(rq * 144) * 8192 + g * 512 + cq * 128, 144 * 8192);
    v4u a[18];
#pragma unroll
    for (int i = 0; i < 18; ++i) a[i] = __builtin_amdgcn_raw_buffer_load_b128(rwq, z * (18 * 8192) + (lane & 7) * 16, i * 8192, AUX_NT);
    SCHED();
    __builtin_amdgcn_s_barrier();
    SCHED();
    STAMP(1);
    unsigned long long e64 = ((unsigned long long)__builtin_amdgcn_readfirstlane(ep.y) << 32) | __builtin_amdgcn_readfirstlane(ep.x);
    e64 += 1;
    const unsigned tl = (unsigned)e64 & 0xffu, th = (unsigned)(e64 >> 8);
    if (lane == 0) { *(LAS v2u*)(L + L_TAG) = v2u{tl, th}; LGKM0(); lds_st(flag + 0, 1u); }
    float fa[8], fb[8];
#pragma unroll
    for (int i = 0; i < 4; ++i) { fa[2 * i] = bflo(xa[i]); fa[2 * i + 1] = bfhi(xa[i]); fb[2 * i] = bflo(xb[i]); fb[2 * i + 1] = bfhi(xb[i]); }
    float m, rden;
    ln_stats(fa, fb, lane, m, rden);
    const Sel sel = make_sel(lane);
    unsigned xr[9] = {xr0.x, xr0.y, xr0.z, xr0.w, xr1.x, xr1.y, xr1.z, xr1.w, xr2};
    f32x4 acc = {0.f, 0.f, 0.f, 0.f};
    STAMP(2);
#pragma unroll
    for (int i = 0; i < 18; ++i) {
      const float xv = (i & 1) ? bfhi(xr[i >> 1]) : bflo(xr[i >> 1]);
      const float t = rbf(xv - m) * rden;
      acc = mfma(a[i], onehot(sel, pk_bf16(t, t)), acc);
    }
    {
      auto r01 = __builtin_amdgcn_permlane32_swap(asu(acc[0]), asu(acc[1]), false, false);
      auto r23 = __builtin_amdgcn_permlane32_swap(asu(acc[2]), asu(acc[3]), false, false);
      const float y0 = asf(r01[0]) + dpp<ROR8>(asf(r01[1])), y1 = asf(r23[0]) + dpp<ROR8>(asf(r23[1]));
      const int pair = cq * 32 + (lane >> 5) * 16 + ((lane >> 4) & 1) * 8 + (lane & 7);
      if (!(lane & 8)) __builtin_amdgcn_raw_buffer_store_b128(v4u{glo(y0, tl), th, glo(y1, tl), th}, rws, pair * 16, WS_Q + g * 8192 + rq * 2048, AUX_SC1);
    }
    STAMP(3);
    __builtin_amdgcn_s_sleep(20);
    for (unsigned spin = 0; spin < SPIN_CAP; ++spin) {
      CBAR();
      v4u q[8];
#pragma unroll
      for (int i = 0; i < 8; ++i) q[i] = __builtin_amdgcn_raw_buffer_load_b128(rws, lane * 16, WS_Q + g * 8192 + i * 1024, AUX_SC1);
      unsigned bad = 0;
#pragma unroll
      for (int i = 0; i < 8; ++i) bad |= gbad(q[i], tl, th);
      if (__builtin_amdgcn_ballot_w64(bad != 0u) == 0ull || spin == SPIN_CAP - 1) {
#pragma unroll
        for (int e = 0; e < 2; ++e) {
          const float v0 = (gval(q[e].x) + gval(q[2 + e].x)) + (gval(q[4 + e].x) + gval(q[6 + e].x));
          const float v1 = (gval(q[e].z) + gval(q[2 + e].z)) + (gval(q[4 + e].z) + gval(q[6 + e].z));
          const unsigned pk = pk_bf16(v0, v1);
          const int c0 = 64 * (2 * e + (lane >> 5)) + 32 * ((lane >> 3) & 1) + 8 * ((lane >> 4) & 1) + (lane & 7);
          *(LAS unsigned short*)(L + L_Q + c0 * 2) = (unsigned short)pk;
          *(LAS unsigned short*)(L + L_Q + c0 * 2 + 32) = (unsigned short)(pk >> 16);
        }
        LGKM0();
        if (lane == 0) lds_st(flag + 1, 1u);
        break;
      }
    }
    STAMP(4);
    float gate = 0.f, xo = 0.f;
    const int col = 64 * (wg >> 4) + 32 * ((wg >> 3) & 1) + 8 * ((wg >> 2) & 1) + 2 * (wg & 3) + ((lane >> 4) & 1) + 16 * (lane >> 5);
    if (wg < 144) {
      xo = bflo(__builtin_amdgcn_raw_buffer_load_b16(rx, col * 2, 0, 0));
      const float bb = bflo(*(const GAS unsigned short*)b2);
      for (unsigned spin = 0; spin < SPIN_CAP; ++spin) {
        CBAR();
        const v2u y = __builtin_amdgcn_raw_buffer_load_b64(rws, col * 128 + (lane & 15) * 8, WS_Y, AUX_SC1);
        const bool live = (lane & 15) < 12;
        const unsigned bad = ((y.x ^ tl) & 0xffu) | (y.y ^ th);
        if (__builtin_amdgcn_ballot_w64(live && bad != 0u) == 0ull || spin == SPIN_CAP - 1) {
          const float yy = rbf(rbf(row_sum(live ? gval(y.x) : 0.f)) + bb);
          gate = rbf(yy / (1.0f + __expf(-yy)));
          break;
        }
        __builtin_amdgcn_s_sleep(8);
      }
    }
    STAMP(5);
    __builtin_amdgcn_s_barrier();
    STAMP(6);
    if (wg < 144) {
      for (unsigned spin = 0; spin < SPIN_CAP; ++spin) {
        CBAR();
        v4u d[8];
#pragma unroll
        for (int i = 0; i < 8; ++i) d[i] = __builtin_amdgcn_raw_buffer_load_b128(rws, lane * 16, WS_S + wg * 8192 + i * 1024, AUX_SC1);
        unsigned bad = 0; float v0 = 0.f, v1 = 0.f;
#pragma unroll
        for (int i = 0; i < 8; ++i) { bad |= gbad(d[i], tl, th); v0 += gval(d[i].x); v1 += gval(d[i].z); }
        if (__builtin_amdgcn_ballot_w64(bad != 0u) == 0ull || spin == SPIN_CAP - 1) {
          auto r = __builtin_amdgcn_permlane32_swap(asu(v0), asu(v1), false, false);
          float y = asf(r[0]) + asf(r[1]);
          y = x16_sum(y); y += dpp<ROR8>(y); y += dpp<ROR4>(y); y += dpp<ROR2>(y);
          const float res = xo + rbf(rbf(y) * gate);
          if ((lane & 15) == ((lane >> 4) & 1)) *(GAS unsigned short*)(out + col * 2) = (unsigned short)pk_bf16(res, res);
          if (wg == 0 && lane == 0) *(GAS unsigned long long*)(ws + WS_E) = e64;
          break;
        }
      }
    }
    STAMP(7);
    STAMP_OUT();
  } else if (w <= 4) {
    const int kw = w - 1, rot = s * 20 + g * 7;
    const __amdgpu_buffer_rsrc_t rk = rsrc(kc + ((size_t)g * 65536 + 61440 + s * 256) * 256, 65536);
    const int voff = ((lane >> 2) & 3) * 256 + ((lane >> 4) * 4 + (lane & 3)) * 16;
    __builtin_amdgcn_s_barrier();
    v4u k[16];
#pragma unroll
    for (int i = 0; i < 16; ++i) k[i] = __builtin_amdgcn_raw_buffer_load_b128(rk, voff, ((kw * 16 + i + rot) & 63) * 1024, AUX_NT);
    SCHED();
    STAMP(1);
    lds_wait(flag + 1);
    STAMP(2);
    const v4u qf = *(LAS v4u*)(L + L_Q + ((lane >> 3) & 1) * 256 + ((lane >> 4) * 4 + (lane & 3)) * 16);
    const v4u zero = {0u, 0u, 0u, 0u};
    const v4u q0 = (lane & 4) ? zero : qf, q1 = (lane & 4) ? qf : zero;
    float sc[8];
    float mx = -INFINITY;
#pragma unroll
    for (int i = 0; i < 8; ++i) {
      f32x4 acc = {0.f, 0.f, 0.f, 0.f};
      acc = mfma(k[2 * i], q0, acc);
      acc = mfma(k[2 * i + 1], q1, acc);
      const float d = (dpp<0x00>(acc[0]) + dpp<0x55>(acc[1])) + (dpp<0xAA>(acc[2]) + dpp<0xFF>(acc[3]));
      sc[i] = rbf(rbf(d) * 0.08838834764831845f);
      mx = fmaxf(mx, sc[i]);
    }
    mx = fmaxf(mx, dpp<HMIR>(mx));
    mx = x32_max(x16_max(mx));
    float l = 0.f; unsigned pp[8];
    const float mo = mx * 1.4426950408889634f;
#pragma unroll
    for (int i = 0; i < 8; ++i) { const float p = __builtin_amdgcn_exp2f(fmaf(sc[i], 1.4426950408889634f, -mo)); l += p; pp[i] = pk_bf16(p, p); }
    l += dpp<HMIR>(l);
    l = x32_sum(x16_sum(l));
    if ((lane & 3) == 0) {
      LAS v4u* pd = (LAS v4u*)(L + L_P + kw * 512 + (lane >> 2) * 32);
      pd[0] = v4u{pp[0], pp[1], pp[2], pp[3]}; pd[1] = v4u{pp[4], pp[5], pp[6], pp[7]};
    }
    if ((lane & 0x37) == 0) *(LAS v2u*)(L + L_ML + kw * 16 + (lane >> 3) * 8) = v2u{asu(mx), asu(l)};
    LGKM0();
    if (lane == 0) lds_st(flag + 2 + kw, 1u);
    STAMP(3);
    STAMP_OUT();
  } else if (w <= 8) {
    const int vw = w - 5, rot = s * 20 + g * 7, h = (lane >> 3) & 1;
    const __amdgpu_buffer_rsrc_t rv = rsrc(vc + ((size_t)g * 65536 + 61440 + s * 256) * 256, 65536);
    __builtin_amdgcn_s_barrier();
    v4u v[16];
#pragma unroll
    for (int i = 0; i < 16; ++i) v[i] = __builtin_amdgcn_raw_buffer_load_b128(rv, lane * 16, ((vw * 16 + i + rot) & 63) * 1024, AUX_NT);
    SCHED();
    STAMP(1);
    lds_wait(flag + 2); lds_wait(flag + 3); lds_wait(flag + 4); lds_wait(flag + 5);
    STAMP(2);
    const Sel sel = make_sel(lane);
    v4u pr[4];
#pragma unroll
    for (int i = 0; i < 4; ++i) pr[i] = *(LAS v4u*)(L + L_P + vw * 512 + (lane >> 3) * 64 + i * 16);
    v2u ml[4];
#pragma unroll
    for (int i = 0; i < 4; ++i) ml[i] = *(LAS v2u*)(L + L_ML + i * 16 + h * 8);
    const v2u mine = *(LAS v2u*)(L + L_ML + vw * 16 + h * 8);
    const v2u tg = *(LAS v2u*)(L + L_TAG);
    const unsigned tl = tg.x, th = tg.y;
    const float M = fmaxf(fmaxf(asf(ml[0].x), asf(ml[1].x)), fmaxf(asf(ml[2].x), asf(ml[3].x)));
    const float f = __expf(asf(mine.x) - M);
    if (vw == 0) {
      float Ls = 0.f;
#pragma unroll
      for (int i = 0; i < 4; ++i) Ls = fmaf(__expf(asf(ml[i].x) - M), asf(ml[i].y), Ls);
      if ((lane & 0x37) == 0) __builtin_amdgcn_raw_buffer_store_b128(v4u{glo(M, tl), th, glo(Ls, tl), th}, rws, h * 256, WS_ML + g * 512 + s * 16, AUX_SC1);
    }
    v4u b[16];
#pragma unroll
    for (int i = 0; i < 16; ++i) { const int idx = (i & 1) * 8 + (i >> 1); b[i] = onehot(sel, pr[idx >> 2][idx & 3]); }
    SCHED();
    f32x4 acc = {0.f, 0.f, 0.f, 0.f};
#pragma unroll
    for (int i = 0; i < 16; ++i) acc = mfma(v[i], b[i], acc);
    acc *= f;
    *(LAS f32x4*)(L + L_RED + vw * 1024 + lane * 16) = acc;
    LGKM0();
    STAMP(3);
    __builtin_amdgcn_s_barrier();
    CBAR();
    if (vw < 2) {
      v2u r[4];
#pragma unroll
      for (int i = 0; i < 4; ++i) r[i] = *(LAS v2u*)(L + L_RED + i * 1024 + lane * 16 + vw * 8);
      const float o0 = (asf(r[0].x) + asf(r[1].x)) + (asf(r[2].x) + asf(r[3].x));
      const float o1 = (asf(r[0].y) + asf(r[1].y)) + (asf(r[2].y) + asf(r[3].y));
      __builtin_amdgcn_raw_buffer_store_b128(v4u{glo(o0, tl), th, glo(o1, tl), th}, rws, (lane >> 2) * 2048 + (lane & 3) * 32 + vw * 16,
                                             WS_O + g * 32768 + s * 128, AUX_SC1);
    }
    STAMP(4);
    STAMP_OUT();
  } else {
    const int ow = w - 9, z = lane >> 3;
    if (ow == 0 && lane < 8) lds_st(flag + lane, 0u);
    const __amdgpu_buffer_rsrc_t rx = rsrc(x, 1152);
    const v4u xa = __builtin_amdgcn_raw_buffer_load_b128(rx, lane * 16, 0, 0);
    const v4u xb = __builtin_amdgcn_raw_buffer_load_b128(rx, lane * 16 + 1024, 0, 0);
    const int cg = wg >> 2, fq = wg & 3;
    const unsigned xc = __builtin_amdgcn_raw_buffer_load_b16(rx, min(lane, 8) * 2, cg * 18, 0);
    const int foff = fq * 2304 + ow * 768;
    const v3u w1v = __builtin_amdgcn_raw_buffer_load_b96(rsrc(w1, 9216), lane * 12, foff, 0);
    const v3u b1v = __builtin_amdgcn_raw_buffer_load_b96(rsrc(b1, 9216), lane * 12, foff, 0);
    const v3u w2v = __builtin_amdgcn_raw_buffer_load_b96(rsrc(w2, 9216), lane * 12, foff, 0);
    LGKM0();
    SCHED();
    __builtin_amdgcn_s_barrier();
    SCHED();
    STAMP(1);
    float fa[8], fb[8];
#pragma unroll
    for (int i = 0; i < 4; ++i) { fa[2 * i] = bflo(xa[i]); fa[2 * i + 1] = bfhi(xa[i]); fb[2 * i] = bflo(xb[i]); fb[2 * i + 1] = bfhi(xb[i]); }
    float m, rden;
    ln_stats(fa, fb, lane, m, rden);
    float hv = ln_apply(bflo(xc), m, rden);
#pragma unroll
    for (int i = 0; i < 8; ++i) { fa[i] = ln_apply(fa[i], m, rden); fb[i] = lane < 8 ? ln_apply(fb[i], m, rden) : 0.f; }
    ln_stats(fa, fb, lane, m, rden);
    hv = ln_apply(hv, m, rden);
    float ysum = 0.f;
    constexpr float C1 = -2.0f * 0.7978845608028654f * 1.4426950408889634f, C2 = C1 * 0.044715f;
#pragma unroll 1
    for (int c = 0; c < 9; ++c) {
      const float hc = asf(__builtin_amdgcn_readlane(asu(hv), c));
      float y = 0.f;
#pragma unroll
      for (int j = 0; j < 3; ++j) {
        const unsigned t = pk_bf16(hc * bflo(w1v[j]), hc * bfhi(w1v[j]));
        const unsigned u = pk_bf16(bflo(t) + bflo(b1v[j]), bfhi(t) + bfhi(b1v[j]));
        const float u0 = bflo(u), u1 = bfhi(u);
        const float e0 = __builtin_amdgcn_exp2f(u0 * fmaf(u0 * u0, C2, C1)), e1 = __builtin_amdgcn_exp2f(u1 * fmaf(u1 * u1, C2, C1));
        const unsigned gl = pk_bf16(u0 * __builtin_amdgcn_rcpf(1.0f + e0), u1 * __builtin_amdgcn_rcpf(1.0f + e1));
        y = fmaf(bflo(gl), bflo(w2v[j]), y); y = fmaf(bfhi(gl), bfhi(w2v[j]), y);
      }
      y = wave_sum(y);
      if (lane == c) ysum = y;
    }
    lds_wait(flag + 0);
    const v2u tg = *(LAS v2u*)(L + L_TAG);
    const unsigned tl = tg.x, th = tg.y;
    if (lane < 9) __builtin_amdgcn_raw_buffer_store_b64(v2u{glo(ysum, tl), th}, rws, lane * 128, WS_Y + cg * 1152 + (fq * 3 + ow) * 8, AUX_SC1);
    STAMP(2);
    const int hh = (s >> 1) & 1;
    const __amdgpu_buffer_rsrc_t rwo = rsrc(wo + (size_t)(g * 256 + hh * 128 + 32 * (s >> 2) + 4 * (s & 1)) * 1152, 28 * 1152);
    v4u a[6];
#pragma unroll
    for (int j = 0; j < 3; ++j)
#pragma unroll
      for (int mm = 0; mm < 2; ++mm)
        a[2 * j + mm] = __builtin_amdgcn_raw_buffer_load_b128(rwo, (16 * (z & 1) + (z >> 1)) * 1152 + (lane & 7) * 16, mm * 8 * 1152 + (ow * 3 + j) * 128, AUX_NT);
    SCHED();
    STAMP(3);
    const Sel sel = make_sel(lane);
    __builtin_amdgcn_s_barrier();
    STAMP(4);
    for (unsigned spin = 0; spin < SPIN_CAP; ++spin) {
      CBAR();
      v4u ml[2], d[2];
#pragma unroll
      for (int t = 0; t < 2; ++t) {
        d[t] = __builtin_amdgcn_raw_buffer_load_b128(rws, (lane & 7) * 128 + z * 16, WS_O + g * 32768 + s * 2048 + t * 1024, AUX_SC1);
        ml[t] = __builtin_amdgcn_raw_buffer_load_b128(rws, (lane & 7) * 16, WS_ML + g * 512 + hh * 256 + t * 128, AUX_SC1);
      }
      const unsigned bad = gbad(d[0], tl, th) | gbad(d[1], tl, th) | gbad(ml[0], tl, th) | gbad(ml[1], tl, th);
      if (__builtin_amdgcn_ballot_w64(bad != 0u) == 0ull || spin == SPIN_CAP - 1) {
        STAMP(5);
        float M = fmaxf(gval(ml[0].x), gval(ml[1].x));
        M = fmaxf(M, dpp<XOR1>(M)); M = fmaxf(M, dpp<XOR2>(M)); M = fmaxf(M, dpp<HMIR>(M));
        const float w0 = __expf(gval(ml[0].x) - M), w1f = __expf(gval(ml[1].x) - M);
        float Ls = w0 * gval(ml[0].z) + w1f * gval(ml[1].z);
        float o0 = w0 * gval(d[0].x) + w1f * gval(d[1].x), o1 = w0 * gval(d[0].z) + w1f * gval(d[1].z);
        Ls += dpp<XOR1>(Ls); o0 += dpp<XOR1>(o0); o1 += dpp<XOR1>(o1);
        Ls += dpp<XOR2>(Ls); o0 += dpp<XOR2>(o0); o1 += dpp<XOR2>(o1);
        Ls += dpp<HMIR>(Ls); o0 += dpp<HMIR>(o0); o1 += dpp<HMIR>(o1);
        const float rl = __builtin_amdgcn_rcpf(Ls);
        o0 *= rl; o1 *= rl;
        const v4u b0 = onehot(sel, pk_bf16(o0, o0)), b1f = onehot(sel, pk_bf16(o1, o1));
        const int voff = (((lane >> 4) & 1) * 8 + (lane >> 5) * 4 + ((lane & 7) >> 1)) * 8192 + (lane & 1) * 16;
#pragma unroll
        for (int j = 0; j < 3; ++j) {
          f32x4 acc = {0.f, 0.f, 0.f, 0.f};
          acc = mfma(a[2 * j], b0, acc);
          acc = mfma(a[2 * j + 1], b1f, acc);
          auto r01 = __builtin_amdgcn_permlane32_swap(asu(acc[0]), asu(acc[1]), false, false);
          auto r23 = __builtin_amdgcn_permlane32_swap(asu(acc[2]), asu(acc[3]), false, false);
          const float y0 = asf(r01[0]) + dpp<ROR8>(asf(r01[1])), y1 = asf(r23[0]) + dpp<ROR8>(asf(r23[1]));
          if (!(lane & 8)) __builtin_amdgcn_raw_buffer_store_b128(v4u{glo(y0, tl), th, glo(y1, tl), th}, rws, voff, WS_S + (ow * 3 + j) * (16 * 8192) + wg * 32, AUX_SC1);
        }
        break;
      }
    }
    STAMP(6);
    STAMP_OUT();
  }
}
}

#include <cstdio>
#include <vector>
#include <algorithm>
__global__ void dbg_scrub(unsigned* p, size_t n, unsigned v) { size_t i = (size_t)blockIdx.x * blockDim.x + threadIdx.x; for (; i < n; i += (size_t)gridDim.x * blockDim.x) p[i] = v + (unsigned)i * 0x9E3779B1u; }
extern "C" void kernel_launch(void* const* d_in, const int* in_sizes, int n_in, void* d_out, int out_size, void* d_ws, size_t ws_size, hipStream_t stream) {
  hipStreamCaptureStatus cs; hipStreamIsCapturing(stream, &cs);
  int reps = 1;
  if (cs == hipStreamCaptureStatusNone) {
    for (int i = 0; i < n_in; ++i) { hipPointerAttribute_t a; hipError_t e = hipPointerGetAttributes(&a, d_in[i]); fprintf(stderr, "in%d %p type %d flags %u err %d\n", i, d_in[i], (int)a.type, a.allocationFlags, (int)e); }
    { hipPointerAttribute_t a; hipError_t e = hipPointerGetAttributes(&a, d_ws); fprintf(stderr, "ws %p type %d flags %u err %d out %p\n", d_ws, (int)a.type, a.allocationFlags, (int)e, d_out); }
    reps = 6;
  }
  for (int r = 0; r < reps; ++r) {
    if (reps > 1 && r > 0) { dbg_scrub<<<2048, 256, 0, stream>>>((unsigned*)d_ws, ws_size / 4, r); hipStreamSynchronize(stream); }
    fused_decode<<<NWG, NTHR, 0, stream>>>((const char*)d_in[0], (const char*)d_in[1], (const char*)d_in[2], (const char*)d_in[3], (const char*)d_in[4],
      (const char*)d_in[6], (const char*)d_in[7], (const char*)d_in[8], (const char*)d_in[9], (char*)d_out, (char*)d_ws);
    if (reps > 1) {
      hipStreamSynchronize(stream);
      std::vector<unsigned> h(256 * 16 * 64);
      hipMemcpy(h.data(), (char*)d_ws + WS_STAMP, h.size() * 4, hipMemcpyDeviceToHost);
      unsigned mn = ~0u; for (int wg = 0; wg < 256; ++wg) for (int w = 0; w < 12; ++w) mn = std::min(mn, h[(wg * 16 + w) * 64]);
      fprintf(stderr, "rep %d:", r);
      for (int w : {0, 1, 5, 9}) { fprintf(stderr, " | w%d", w); for (int j = 0; j < 8; ++j) { double a = 0, m = 0; int c = 0; for (int wg = 0; wg < 256; ++wg) { unsigned t = h[(wg * 16 + w) * 64 + j]; if (t) { double d = (t - mn) * 0.01; a += d; m = std::max(m, d); ++c; } } if (c) fprintf(stderr, " %.1f/%.1f", a / c, m); } }
      fprintf(stderr, "\n");
    }
  }
}
```

```cpp
#include <hip/hip_runtime.h>
#include <stdint.h>

#define STAMPS 0

namespace {
typedef unsigned v4u __attribute__((ext_vector_type(4)));
typedef unsigned v3u __attribute__((ext_vector_type(3)));
typedef unsigned v2u __attribute__((ext_vector_type(2)));
typedef float f32x4 __attribute__((ext_vector_type(4)));
typedef short bf16x8 __attribute__((ext_vector_type(8)));
typedef __bf16 bf16x2 __attribute__((ext_vector_type(2)));
#define LAS __attribute__((address_space(3)))
#define GAS __attribute__((address_space(1)))

constexpr int AUX_NT = 2, AUX_SC1 = 16, RSRC3 = 0x00020000;
constexpr int NWG = 256, NTHR = 768;
constexpr int WS_E = 0;
constexpr int WS_Q = 4096;
constexpr int WS_ML = WS_Q + 16 * 8192;
constexpr int WS_Y = WS_ML + 16 * 512;
constexpr int WS_O = WS_Y + 576 * 128;
constexpr int WS_S = WS_O + 256 * 2048;
constexpr int WS_END = WS_S + 144 * 8192;
constexpr int WS_STAMP = 4 << 20;
constexpr unsigned SPIN_CAP = 1u << 18;

constexpr int L_FLAG = 0;
constexpr int L_TAG = 32;
constexpr int L_Q = 64;
constexpr int L_ML = 576;
constexpr int L_P = 640;
constexpr int L_RED = L_P + 2048;
constexpr int L_BYTES = L_RED + 4096;

__device__ __forceinline__ float asf(unsigned u) { return __builtin_bit_cast(float, u); }
__device__ __forceinline__ unsigned asu(float f) { return __builtin_bit_cast(unsigned, f); }
__device__ __forceinline__ float bflo(unsigned u) { return asf(u << 16); }
__device__ __forceinline__ float bfhi(unsigned u) { return asf(u & 0xffff0000u); }
__device__ __forceinline__ unsigned pk_bf16(float lo, float hi) {
  bf16x2 v = {(__bf16)lo, (__bf16)hi}; return __builtin_bit_cast(unsigned, v); }
__device__ __forceinline__ float rbf(float f) { return bflo(pk_bf16(f, f)); }
template <int CTRL> __device__ __forceinline__ float dpp(float x) {
  return asf(__builtin_amdgcn_mov_dpp(asu(x), CTRL, 0xf, 0xf, true)); }
constexpr int XOR1 = 0xB1, XOR2 = 0x4E, HMIR = 0x141, ROR8 = 0x128, ROR4 = 0x124, ROR2 = 0x122, ROR1 = 0x121;
__device__ __forceinline__ float x16_sum(float x) { auto s = __builtin_amdgcn_permlane16_swap(asu(x), asu(x), false, false); return asf(s[0]) + asf(s[1]); }
__device__ __forceinline__ float x32_sum(float x) { auto s = __builtin_amdgcn_permlane32_swap(asu(x), asu(x), false, false); return asf(s[0]) + asf(s[1]); }
__device__ __forceinline__ float x16_max(float x) { auto s = __builtin_amdgcn_permlane16_swap(asu(x), asu(x), false, false); return fmaxf(asf(s[0]), asf(s[1])); }
__device__ __forceinline__ float x32_max(float x) { auto s = __builtin_amdgcn_permlane32_swap(asu(x), asu(x), false, false); return fmaxf(asf(s[0]), asf(s[1])); }
__device__ __forceinline__ float row_sum(float x) { x += dpp<ROR8>(x); x += dpp<ROR4>(x); x += dpp<ROR2>(x); x += dpp<ROR1>(x); return x; }
__device__ __forceinline__ float wave_sum(float x) { return x32_sum(x16_sum(row_sum(x))); }
__device__ __forceinline__ f32x4 mfma(v4u a, v4u b, f32x4 c) {
  return __builtin_amdgcn_mfma_f32_16x16x32_bf16(__builtin_bit_cast(bf16x8, a), __builtin_bit_cast(bf16x8, b), c, 0, 0, 0); }
__device__ __forceinline__ __amdgpu_buffer_rsrc_t rsrc(const void* p, int bytes) { return __builtin_amdgcn_make_buffer_rsrc((void*)p, 0, bytes, RSRC3); }
#define SCHED() __builtin_amdgcn_sched_barrier(0)
#define CBAR() asm volatile("" ::: "memory")
#define LGKM0() asm volatile("s_waitcnt lgkmcnt(0)" ::: "memory")
__device__ __forceinline__ unsigned lds_ld(LAS unsigned* p) { return __hip_atomic_load(p, __ATOMIC_RELAXED, __HIP_MEMORY_SCOPE_WORKGROUP); }
__device__ __forceinline__ void lds_st(LAS unsigned* p, unsigned v) { __hip_atomic_store(p, v, __ATOMIC_RELAXED, __HIP_MEMORY_SCOPE_WORKGROUP); }
__device__ __forceinline__ void lds_wait(LAS unsigned* p) {
  for (unsigned i = 0; i < (SPIN_CAP << 4); ++i) { if (__builtin_amdgcn_readfirstlane(lds_ld(p)) != 0u) break; __builtin_amdgcn_s_sleep(1); }
  CBAR();
}
struct Sel { unsigned m[4]; };
__device__ __forceinline__ Sel make_sel(int lane) { Sel s; const unsigned h = (lane & 1) ? 0xffff0000u : 0x0000ffffu;
#pragma unroll
  for (int r = 0; r < 4; ++r) s.m[r] = (((lane >> 1) & 3) == r) ? h : 0u;
  return s; }
__device__ __forceinline__ v4u onehot(const Sel& s, unsigned pp) { return v4u{pp & s.m[0], pp & s.m[1], pp & s.m[2], pp & s.m[3]}; }
__device__ __forceinline__ unsigned glo(float v, unsigned tl) { return (asu(v) & 0xffffff00u) | tl; }
__device__ __forceinline__ float gval(unsigned lo) { return asf(lo & 0xffffff00u); }
__device__ __forceinline__ unsigned gbad(v4u g, unsigned tl, unsigned th) { return (((g.x ^ tl) | (g.z ^ tl)) & 0xffu) | (g.y ^ th) | (g.w ^ th); }

__device__ __forceinline__ void ln_stats(const float (&a)[8], const float (&b)[8], int lane, float& m, float& rden) {
  float s = 0.f;
#pragma unroll
  for (int i = 0; i < 8; ++i) s += a[i] + b[i];
  const float mean = wave_sum(s) * (1.0f / 576.0f);
  const float mb = lane < 8 ? mean : 0.f;
  float v = 0.f;
#pragma unroll
  for (int i = 0; i < 8; ++i) { const float d = a[i] - mean, e = b[i] - mb; v = fmaf(d, d, v); v = fmaf(e, e, v); }
  const float var = wave_sum(v) * (1.0f / 576.0f);
  m = rbf(mean);
  const float den = rbf(__builtin_sqrtf(rbf(rbf(var) + 1e-5f)));
  rden = 1.0f / den;
}
__device__ __forceinline__ float ln_apply(float x, float m, float rden) { return rbf(rbf(x - m) * rden); }

#define PROBE_W 0
#define PROBE_S 0
#define STAMP(i) do { if ((i) == 0 || (i) == PROBE_S) { SCHED(); unsigned long long t_; asm volatile("s_memrealtime %0\n\ts_waitcnt lgkmcnt(0)" : "=s"(t_) :: "memory"); if ((i) == 0) pt0 = t_; if ((i) == PROBE_S) pt1 = t_; SCHED(); } } while (0)
#define STAMP_OUT() do { if (w == PROBE_W) { const unsigned long long until = pt0 + 4000ull + 10ull * (pt1 - pt0); for (int it_ = 0; it_ < 2000000; ++it_) { if (__builtin_amdgcn_s_memrealtime() >= until) break; __builtin_amdgcn_s_sleep(2); } } } while (0)

__global__ void __launch_bounds__(NTHR) fused_decode(const char* __restrict__ x, const char* __restrict__ kc, const char* __restrict__ vc,
    const char* __restrict__ wq, const char* __restrict__ wo, const char* __restrict__ w1, const char* __restrict__ b1,
    const char* __restrict__ w2, const char* __restrict__ b2, char* __restrict__ out, char* __restrict__ ws) {
  __shared__ __attribute__((aligned(16))) unsigned char smem[L_BYTES];
  LAS unsigned char* L = (LAS unsigned char*)smem;
  LAS unsigned* flag = (LAS unsigned*)(L + L_FLAG);
  const int wg = blockIdx.x, lane = threadIdx.x & 63, w = __builtin_amdgcn_readfirstlane(threadIdx.x >> 6);
  const int g = (wg & 7) * 2 + (wg >> 7), s = (wg >> 3) & 15;
  unsigned long long pt0 = 0, pt1 = 0;
  STAMP(0);
  const __amdgpu_buffer_rsrc_t rws = rsrc(ws, WS_END);

  if (w == 0) {
    const int cq = s & 3, rq = s >> 2, z = lane >> 3;
    const __amdgpu_buffer_rsrc_t rx = rsrc(x, 1152);
    const v2u ep = __builtin_amdgcn_raw_buffer_load_b64(rws, 0, WS_E, AUX_SC1);
    const v4u xa = __builtin_amdgcn_raw_buffer_load_b128(rx, lane * 16, 0, 0);
    const v4u xb = __builtin_amdgcn_raw_buffer_load_b128(rx, lane * 16 + 1024, 0, 0);
    const v4u xr0 = __builtin_amdgcn_raw_buffer_load_b128(rx, z * 36, rq * 288, 0);
    const v4u xr1 = __builtin_amdgcn_raw_buffer_load_b128(rx, z * 36 + 16, rq * 288, 0);
    const unsigned xr2 = __builtin_amdgcn_raw_buffer_load_b32(rx, z * 36 + 32, rq * 288, 0);
    const __amdgpu_buffer_rsrc_t rwq = rsrc(wq + (size_t)(rq * 144) * 8192 + g * 512 + cq * 128, 144 * 8192);
    v4u a[18];
#pragma unroll
    for (int i = 0; i < 18; ++i) a[i] = __builtin_amdgcn_raw_buffer_load_b128(rwq, z * (18 * 8192) + (lane & 7) * 16, i * 8192, AUX_NT);
    SCHED();
    __builtin_amdgcn_s_barrier();
    SCHED();
    STAMP(1);
    unsigned long long e64 = ((unsigned long long)__builtin_amdgcn_readfirstlane(ep.y) << 32) | __builtin_amdgcn_readfirstlane(ep.x);
    e64 += 1;
    const unsigned tl = (unsigned)e64 & 0xffu, th = (unsigned)(e64 >> 8);
    if (lane == 0) { *(LAS v2u*)(L + L_TAG) = v2u{tl, th}; LGKM0(); lds_st(flag + 0, 1u); }
    float fa[8], fb[8];
#pragma unroll
    for (int i = 0; i < 4; ++i) { fa[2 * i] = bflo(xa[i]); fa[2 * i + 1] = bfhi(xa[i]); fb[2 * i] = bflo(xb[i]); fb[2 * i + 1] = bfhi(xb[i]); }
    float m, rden;
    ln_stats(fa, fb, lane, m, rden);
    const Sel sel = make_sel(lane);
    unsigned xr[9] = {xr0.x, xr0.y, xr0.z, xr0.w, xr1.x, xr1.y, xr1.z, xr1.w, xr2};
    f32x4 acc = {0.f, 0.f, 0.f, 0.f};
    STAMP(2);
#pragma unroll
    for (int i = 0; i < 18; ++i) {
      const float xv = (i & 1) ? bfhi(xr[i >> 1]) : bflo(xr[i >> 1]);
      const float t = rbf(xv - m) * rden;
      acc = mfma(a[i], onehot(sel, pk_bf16(t, t)), acc);
    }
    {
      auto r01 = __builtin_amdgcn_permlane32_swap(asu(acc[0]), asu(acc[1]), false, false);
      auto r23 = __builtin_amdgcn_permlane32_swap(asu(acc[2]), asu(acc[3]), false, false);
      const float y0 = asf(r01[0]) + dpp<ROR8>(asf(r01[1])), y1 = asf(r23[0]) + dpp<ROR8>(asf(r23[1]));
      const int pair = cq * 32 + (lane >> 5) * 16 + ((lane >> 4) & 1) * 8 + (lane & 7);
      if (!(lane & 8)) __builtin_amdgcn_raw_buffer_store_b128(v4u{glo(y0, tl), th, glo(y1, tl), th}, rws, pair * 16, WS_Q + g * 8192 + rq * 2048, AUX_SC1);
    }
    STAMP(3);
    __builtin_amdgcn_s_sleep(20);
    for (unsigned spin = 0; spin < SPIN_CAP; ++spin) {
      CBAR();
      v4u q[8];
#pragma unroll
      for (int i = 0; i < 8; ++i) q[i] = __builtin_amdgcn_raw_buffer_load_b128(rws, lane * 16, WS_Q + g * 8192 + i * 1024, AUX_SC1);
      unsigned bad = 0;
#pragma unroll
      for (int i = 0; i < 8; ++i) bad |= gbad(q[i], tl, th);
      if (__builtin_amdgcn_ballot_w64(bad != 0u) == 0ull || spin == SPIN_CAP - 1) {
#pragma unroll
        for (int e = 0; e < 2; ++e) {
          const float v0 = (gval(q[e].x) + gval(q[2 + e].x)) + (gval(q[4 + e].x) + gval(q[6 + e].x));
          const float v1 = (gval(q[e].z) + gval(q[2 + e].z)) + (gval(q[4 + e].z) + gval(q[6 + e].z));
          const unsigned pk = pk_bf16(v0, v1);
          const int c0 = 64 * (2 * e + (lane >> 5)) + 32 * ((lane >> 3) & 1) + 8 * ((lane >> 4) & 1) + (lane & 7);
          *(LAS unsigned short*)(L + L_Q + c0 * 2) = (unsigned short)pk;
          *(LAS unsigned short*)(L + L_Q + c0 * 2 + 32) = (unsigned short)(pk >> 16);
        }
        LGKM0();
        if (lane == 0) lds_st(flag + 1, 1u);
        break;
      }
    }
    STAMP(4);
    float gate = 0.f, xo = 0.f;
    const int col = 64 * (wg >> 4) + 32 * ((wg >> 3) & 1) + 8 * ((wg >> 2) & 1) + 2 * (wg & 3) + ((lane >> 4) & 1) + 16 * (lane >> 5);
    if (wg < 144) {
      xo = bflo(__builtin_amdgcn_raw_buffer_load_b16(rx, col * 2, 0, 0));
      const float bb = bflo(*(const GAS unsigned short*)b2);
      for (unsigned spin = 0; spin < SPIN_CAP; ++spin) {
        CBAR();
        const v2u y = __builtin_amdgcn_raw_buffer_load_b64(rws, col * 128 + (lane & 15) * 8, WS_Y, AUX_SC1);
        const bool live = (lane & 15) < 12;
        const unsigned bad = ((y.x ^ tl) & 0xffu) | (y.y ^ th);
        if (__builtin_amdgcn_ballot_w64(live && bad != 0u) == 0ull || spin == SPIN_CAP - 1) {
          const float yy = rbf(rbf(row_sum(live ? gval(y.x) : 0.f)) + bb);
          gate = rbf(yy / (1.0f + __expf(-yy)));
          break;
        }
        __builtin_amdgcn_s_sleep(8);
      }
    }
    STAMP(5);
    __builtin_amdgcn_s_barrier();
    STAMP(6);
    if (wg < 144) {
      for (unsigned spin = 0; spin < SPIN_CAP; ++spin) {
        CBAR();
        v4u d[8];
#pragma unroll
        for (int i = 0; i < 8; ++i) d[i] = __builtin_amdgcn_raw_buffer_load_b128(rws, lane * 16, WS_S + wg * 8192 + i * 1024, AUX_SC1);
        unsigned bad = 0; float v0 = 0.f, v1 = 0.f;
#pragma unroll
        for (int i = 0; i < 8; ++i) { bad |= gbad(d[i], tl, th); v0 += gval(d[i].x); v1 += gval(d[i].z); }
        if (__builtin_amdgcn_ballot_w64(bad != 0u) == 0ull || spin == SPIN_CAP - 1) {
          auto r = __builtin_amdgcn_permlane32_swap(asu(v0), asu(v1), false, false);
          float y = asf(r[0]) + asf(r[1]);
          y = x16_sum(y); y += dpp<ROR8>(y); y += dpp<ROR4>(y); y += dpp<ROR2>(y);
          const float res = xo + rbf(rbf(y) * gate);
          if ((lane & 15) == ((lane >> 4) & 1)) *(GAS unsigned short*)(out + col * 2) = (unsigned short)pk_bf16(res, res);
          if (wg == 0 && lane == 0) *(GAS unsigned long long*)(ws + WS_E) = e64;
          break;
        }
      }
    }
    STAMP(7);
    STAMP_OUT();
  } else if (w <= 4) {
    const int kw = w - 1, rot = s * 20 + g * 7;
    const __amdgpu_buffer_rsrc_t rk = rsrc(kc + ((size_t)g * 65536 + 61440 + s * 256) * 256, 65536);
    const int voff = ((lane >> 2) & 3) * 256 + ((lane >> 4) * 4 + (lane & 3)) * 16;
    __builtin_amdgcn_s_barrier();
    v4u k[16];
#pragma unroll
    for (int i = 0; i < 16; ++i) k[i] = __builtin_amdgcn_raw_buffer_load_b128(rk, voff, ((kw * 16 + i + rot) & 63) * 1024, AUX_NT);
    SCHED();
    STAMP(1);
    lds_wait(flag + 1);
    STAMP(2);
    const v4u qf = *(LAS v4u*)(L + L_Q + ((lane >> 3) & 1) * 256 + ((lane >> 4) * 4 + (lane & 3)) * 16);
    const v4u zero = {0u, 0u, 0u, 0u};
    const v4u q0 = (lane & 4) ? zero : qf, q1 = (lane & 4) ? qf : zero;
    float sc[8];
    float mx = -INFINITY;
#pragma unroll
    for (int i = 0; i < 8; ++i) {
      f32x4 acc = {0.f, 0.f, 0.f, 0.f};
      acc = mfma(k[2 * i], q0, acc);
      acc = mfma(k[2 * i + 1], q1, acc);
      const float d = (dpp<0x00>(acc[0]) + dpp<0x55>(acc[1])) + (dpp<0xAA>(acc[2]) + dpp<0xFF>(acc[3]));
      sc[i] = rbf(rbf(d) * 0.08838834764831845f);
      mx = fmaxf(mx, sc[i]);
    }
    mx = fmaxf(mx, dpp<HMIR>(mx));
    mx = x32_max(x16_max(mx));
    float l = 0.f; unsigned pp[8];
    const float mo = mx * 1.4426950408889634f;
#pragma unroll
    for (int i = 0; i < 8; ++i) { const float p = __builtin_amdgcn_exp2f(fmaf(sc[i], 1.4426950408889634f, -mo)); l += p; pp[i] = pk_bf16(p, p); }
    l += dpp<HMIR>(l);
    l = x32_sum(x16_sum(l));
    if ((lane & 3) == 0) {
      LAS v4u* pd = (LAS v4u*)(L + L_P + kw * 512 + (lane >> 2) * 32);
      pd[0] = v4u{pp[0], pp[1], pp[2], pp[3]}; pd[1] = v4u{pp[4], pp[5], pp[6], pp[7]};
    }
    if ((lane & 0x37) == 0) *(LAS v2u*)(L + L_ML + kw * 16 + (lane >> 3) * 8) = v2u{asu(mx), asu(l)};
    LGKM0();
    if (lane == 0) lds_st(flag + 2 + kw, 1u);
    STAMP(3);
    STAMP_OUT();
  } else if (w <= 8) {
    const int vw = w - 5, rot = s * 20 + g * 7, h = (lane >> 3) & 1;
    const __amdgpu_buffer_rsrc_t rv = rsrc(vc + ((size_t)g * 65536 + 61440 + s * 256) * 256, 65536);
    __builtin_amdgcn_s_barrier();
    v4u v[16];
#pragma unroll
    for (int i = 0; i < 16; ++i) v[i] = __builtin_amdgcn_raw_buffer_load_b128(rv, lane * 16, ((vw * 16 + i + rot) & 63) * 1024, AUX_NT);
    SCHED();
    STAMP(1);
    lds_wait(flag + 2); lds_wait(flag + 3); lds_wait(flag + 4); lds_wait(flag + 5);
    STAMP(2);
    const Sel sel = make_sel(lane);
    v4u pr[4];
#pragma unroll
    for (int i = 0; i < 4; ++i) pr[i] = *(LAS v4u*)(L + L_P + vw * 512 + (lane >> 3) * 64 + i * 16);
    v2u ml[4];
#pragma unroll
    for (int i = 0; i < 4; ++i) ml[i] = *(LAS v2u*)(L + L_ML + i * 16 + h * 8);
    const v2u mine = *(LAS v2u*)(L + L_ML + vw * 16 + h * 8);
    const v2u tg = *(LAS v2u*)(L + L_TAG);
    const unsigned tl = tg.x, th = tg.y;
    const float M = fmaxf(fmaxf(asf(ml[0].x), asf(ml[1].x)), fmaxf(asf(ml[2].x), asf(ml[3].x)));
    const float f = __expf(asf(mine.x) - M);
    if (vw == 0) {
      float Ls = 0.f;
#pragma unroll
      for (int i = 0; i < 4; ++i) Ls = fmaf(__expf(asf(ml[i].x) - M), asf(ml[i].y), Ls);
      if ((lane & 0x37) == 0) __builtin_amdgcn_raw_buffer_store_b128(v4u{glo(M, tl), th, glo(Ls, tl), th}, rws, h * 256, WS_ML + g * 512 + s * 16, AUX_SC1);
    }
    v4u b[16];
#pragma unroll
    for (int i = 0; i < 16; ++i) { const int idx = (i & 1) * 8 + (i >> 1); b[i] = onehot(sel, pr[idx >> 2][idx & 3]); }
    SCHED();
    f32x4 acc = {0.f, 0.f, 0.f, 0.f};
#pragma unroll
    for (int i = 0; i < 16; ++i) acc = mfma(v[i], b[i], acc);
    acc *= f;
    *(LAS f32x4*)(L + L_RED + vw * 1024 + lane * 16) = acc;
    LGKM0();
    STAMP(3);
    __builtin_amdgcn_s_barrier();
    CBAR();
    if (vw < 2) {
      v2u r[4];
#pragma unroll
      for (int i = 0; i < 4; ++i) r[i] = *(LAS v2u*)(L + L_RED + i * 1024 + lane * 16 + vw * 8);
      const float o0 = (asf(r[0].x) + asf(r[1].x)) + (asf(r[2].x) + asf(r[3].x));
      const float o1 = (asf(r[0].y) + asf(r[1].y)) + (asf(r[2].y) + asf(r[3].y));
      __builtin_amdgcn_raw_buffer_store_b128(v4u{glo(o0, tl), th, glo(o1, tl), th}, rws, (lane >> 2) * 2048 + (lane & 3) * 32 + vw * 16,
                                             WS_O + g * 32768 + s * 128, AUX_SC1);
    }
    STAMP(4);
    STAMP_OUT();
  } else {
    const int ow = w - 9, z = lane >> 3;
    if (ow == 0 && lane < 8) lds_st(flag + lane, 0u);
    const __amdgpu_buffer_rsrc_t rx = rsrc(x, 1152);
    const v4u xa = __builtin_amdgcn_raw_buffer_load_b128(rx, lane * 16, 0, 0);
    const v4u xb = __builtin_amdgcn_raw_buffer_load_b128(rx, lane * 16 + 1024, 0, 0);
    const int cg = wg >> 2, fq = wg & 3;
    const unsigned xc = __builtin_amdgcn_raw_buffer_load_b16(rx, min(lane, 8) * 2, cg * 18, 0);
    const int foff = fq * 2304 + ow * 768;
    const v3u w1v = __builtin_amdgcn_raw_buffer_load_b96(rsrc(w1, 9216), lane * 12, foff, 0);
    const v3u b1v = __builtin_amdgcn_raw_buffer_load_b96(rsrc(b1, 9216), lane * 12, foff, 0);
    const v3u w2v = __builtin_amdgcn_raw_buffer_load_b96(rsrc(w2, 9216), lane * 12, foff, 0);
    LGKM0();
    SCHED();
    __builtin_amdgcn_s_barrier();
    SCHED();
    STAMP(1);
    float fa[8], fb[8];
#pragma unroll
    for (int i = 0; i < 4; ++i) { fa[2 * i] = bflo(xa[i]); fa[2 * i + 1] = bfhi(xa[i]); fb[2 * i] = bflo(xb[i]); fb[2 * i + 1] = bfhi(xb[i]); }
    float m, rden;
    ln_stats(fa, fb, lane, m, rden);
    float hv = ln_apply(bflo(xc), m, rden);
#pragma unroll
    for (int i = 0; i < 8; ++i) { fa[i] = ln_apply(fa[i], m, rden); fb[i] = lane < 8 ? ln_apply(fb[i], m, rden) : 0.f; }
    ln_stats(fa, fb, lane, m, rden);
    hv = ln_apply(hv, m, rden);
    float ysum = 0.f;
    constexpr float C1 = -2.0f * 0.7978845608028654f * 1.4426950408889634f, C2 = C1 * 0.044715f;
#pragma unroll 1
    for (int c = 0; c < 9; ++c) {
      const float hc = asf(__builtin_amdgcn_readlane(asu(hv), c));
      float y = 0.f;
#pragma unroll
      for (int j = 0; j < 3; ++j) {
        const unsigned t = pk_bf16(hc * bflo(w1v[j]), hc * bfhi(w1v[j]));
        const unsigned u = pk_bf16(bflo(t) + bflo(b1v[j]), bfhi(t) + bfhi(b1v[j]));
        const float u0 = bflo(u), u1 = bfhi(u);
        const float e0 = __builtin_amdgcn_exp2f(u0 * fmaf(u0 * u0, C2, C1)), e1 = __builtin_amdgcn_exp2f(u1 * fmaf(u1 * u1, C2, C1));
        const unsigned gl = pk_bf16(u0 * __builtin_amdgcn_rcpf(1.0f + e0), u1 * __builtin_amdgcn_rcpf(1.0f + e1));
        y = fmaf(bflo(gl), bflo(w2v[j]), y); y = fmaf(bfhi(gl), bfhi(w2v[j]), y);
      }
      y = wave_sum(y);
      if (lane == c) ysum = y;
    }
    lds_wait(flag + 0);
    const v2u tg = *(LAS v2u*)(L + L_TAG);
    const unsigned tl = tg.x, th = tg.y;
    if (lane < 9) __builtin_amdgcn_raw_buffer_store_b64(v2u{glo(ysum, tl), th}, rws, lane * 128, WS_Y + cg * 1152 + (fq * 3 + ow) * 8, AUX_SC1);
    STAMP(2);
    const int hh = (s >> 1) & 1;
    const __amdgpu_buffer_rsrc_t rwo = rsrc(wo + (size_t)(g * 256 + hh * 128 + 32 * (s >> 2) + 4 * (s & 1)) * 1152, 28 * 1152);
    v4u a[6];
#pragma unroll
    for (int j = 0; j < 3; ++j)
#pragma unroll
      for (int mm = 0; mm < 2; ++mm)
        a[2 * j + mm] = __builtin_amdgcn_raw_buffer_load_b128(rwo, (16 * (z & 1) + (z >> 1)) * 1152 + (lane & 7) * 16, mm * 8 * 1152 + (ow * 3 + j) * 128, AUX_NT);
    SCHED();
    STAMP(3);
    const Sel sel = make_sel(lane);
    __builtin_amdgcn_s_barrier();
    STAMP(4);
    for (unsigned spin = 0; spin < SPIN_CAP; ++spin) {
      CBAR();
      v4u ml[2], d[2];
#pragma unroll
      for (int t = 0; t < 2; ++t) {
        d[t] = __builtin_amdgcn_raw_buffer_load_b128(rws, (lane & 7) * 128 + z * 16, WS_O + g * 32768 + s * 2048 + t * 1024, AUX_SC1);
        ml[t] = __builtin_amdgcn_raw_buffer_load_b128(rws, (lane & 7) * 16, WS_ML + g * 512 + hh * 256 + t * 128, AUX_SC1);
      }
      const unsigned bad = gbad(d[0], tl, th) | gbad(d[1], tl, th) | gbad(ml[0], tl, th) | gbad(ml[1], tl, th);
      if (__builtin_amdgcn_ballot_w64(bad != 0u) == 0ull || spin == SPIN_CAP - 1) {
        STAMP(5);
        float M = fmaxf(gval(ml[0].x), gval(ml[1].x));
        M = fmaxf(M, dpp<XOR1>(M)); M = fmaxf(M, dpp<XOR2>(M)); M = fmaxf(M, dpp<HMIR>(M));
        const float w0 = __expf(gval(ml[0].x) - M), w1f = __expf(gval(ml[1].x) - M);
        float Ls = w0 * gval(ml[0].z) + w1f * gval(ml[1].z);
        float o0 = w0 * gval(d[0].x) + w1f * gval(d[1].x), o1 = w0 * gval(d[0].z) + w1f * gval(d[1].z);
        Ls += dpp<XOR1>(Ls); o0 += dpp<XOR1>(o0); o1 += dpp<XOR1>(o1);
        Ls += dpp<XOR2>(Ls); o0 += dpp<XOR2>(o0); o1 += dpp<XOR2>(o1);
        Ls += dpp<HMIR>(Ls); o0 += dpp<HMIR>(o0); o1 += dpp<HMIR>(o1);
        const float rl = __builtin_amdgcn_rcpf(Ls);
        o0 *= rl; o1 *= rl;
        const v4u b0 = onehot(sel, pk_bf16(o0, o0)), b1f = onehot(sel, pk_bf16(o1, o1));
        const int voff = (((lane >> 4) & 1) * 8 + (lane >> 5) * 4 + ((lane & 7) >> 1)) * 8192 + (lane & 1) * 16;
#pragma unroll
        for (int j = 0; j < 3; ++j) {
          f32x4 acc = {0.f, 0.f, 0.f, 0.f};
          acc = mfma(a[2 * j], b0, acc);
          acc = mfma(a[2 * j + 1], b1f, acc);
          auto r01 = __builtin_amdgcn_permlane32_swap(asu(acc[0]), asu(acc[1]), false, false);
          auto r23 = __builtin_amdgcn_permlane32_swap(asu(acc[2]), asu(acc[3]), false, false);
          const float y0 = asf(r01[0]) + dpp<ROR8>(asf(r01[1])), y1 = asf(r23[0]) + dpp<ROR8>(asf(r23[1]));
          if (!(lane & 8)) __builtin_amdgcn_raw_buffer_store_b128(v4u{glo(y0, tl), th, glo(y1, tl), th}, rws, voff, WS_S + (ow * 3 + j) * (16 * 8192) + wg * 32, AUX_SC1);
        }
        break;
      }
    }
    STAMP(6);
    STAMP_OUT();
  }
}
}

extern "C" void kernel_launch(void* const* d_in, const int* in_sizes, int n_in, void* d_out, int out_size, void* d_ws, size_t ws_size, hipStream_t stream) {
  fused_decode<<<NWG, NTHR, 0, stream>>>((const char*)d_in[0], (const char*)d_in[1], (const char*)d_in[2], (const char*)d_in[3], (const char*)d_in[4],
      (const char*)d_in[6], (const char*)d_in[7], (const char*)d_in[8], (const char*)d_in[9], (char*)d_out, (char*)d_ws);
}
```

```cpp
#include <hip/hip_runtime.h>
#include <stdint.h>

#define STAMPS 0

namespace {
typedef unsigned v4u __attribute__((ext_vector_type(4)));
typedef unsigned v3u __attribute__((ext_vector_type(3)));
typedef unsigned v2u __attribute__((ext_vector_type(2)));
typedef float f32x4 __attribute__((ext_vector_type(4)));
typedef short bf16x8 __attribute__((ext_vector_type(8)));
typedef __bf16 bf16x2 __attribute__((ext_vector_type(2)));
#define LAS __attribute__((address_space(3)))
#define GAS __attribute__((address_space(1)))

constexpr int AUX_NT = 2, AUX_SC1 = 16, RSRC3 = 0x00020000;
constexpr int NWG = 256, NTHR = 768;
constexpr int WS_E = 0;
constexpr int WS_Q = 4096;
constexpr int WS_ML = WS_Q + 16 * 8192;
constexpr int WS_Y = WS_ML + 16 * 512;
constexpr int WS_O = WS_Y + 576 * 128;
constexpr int WS_S = WS_O + 256 * 2048;
constexpr int WS_END = WS_S + 144 * 8192;
constexpr int WS_STAMP = 4 << 20;
constexpr unsigned SPIN_CAP = 1u << 18;

constexpr int L_FLAG = 0;
constexpr int L_TAG = 32;
constexpr int L_Q = 64;
constexpr int L_ML = 576;
constexpr int L_P = 640;
constexpr int L_RED = L_P + 2048;
constexpr int L_BYTES = L_RED + 4096;

__device__ __forceinline__ float asf(unsigned u) { return __builtin_bit_cast(float, u); }
__device__ __forceinline__ unsigned asu(float f) { return __builtin_bit_cast(unsigned, f); }
__device__ __forceinline__ float bflo(unsigned u) { return asf(u << 16); }
__device__ __forceinline__ float bfhi(unsigned u) { return asf(u & 0xffff0000u); }
__device__ __forceinline__ unsigned pk_bf16(float lo, float hi) {
  bf16x2 v = {(__bf16)lo, (__bf16)hi}; return __builtin_bit_cast(unsigned, v); }
__device__ __forceinline__ float rbf(float f) { return bflo(pk_bf16(f, f)); }
template <int CTRL> __device__ __forceinline__ float dpp(float x) {
  return asf(__builtin_amdgcn_mov_dpp(asu(x), CTRL, 0xf, 0xf, true)); }
constexpr int XOR1 = 0xB1, XOR2 = 0x4E, HMIR = 0x141, ROR8 = 0x128, ROR4 = 0x124, ROR2 = 0x122, ROR1 = 0x121;
__device__ __forceinline__ float x16_sum(float x) { auto s = __builtin_amdgcn_permlane16_swap(asu(x), asu(x), false, false); return asf(s[0]) + asf(s[1]); }
__device__ __forceinline__ float x32_sum(float x) { auto s = __builtin_amdgcn_permlane32_swap(asu(x), asu(x), false, false); return asf(s[0]) + asf(s[1]); }
__device__ __forceinline__ float x16_max(float x) { auto s = __builtin_amdgcn_permlane16_swap(asu(x), asu(x), false, false); return fmaxf(asf(s[0]), asf(s[1])); }
__device__ __forceinline__ float x32_max(float x) { auto s = __builtin_amdgcn_permlane32_swap(asu(x), asu(x), false, false); return fmaxf(asf(s[0]), asf(s[1])); }
__device__ __forceinline__ float row_sum(float x) { x += dpp<ROR8>(x); x += dpp<ROR4>(x); x += dpp<ROR2>(x); x += dpp<ROR1>(x); return x; }
__device__ __forceinline__ float wave_sum(float x) { return x32_sum(x16_sum(row_sum(x))); }
__device__ __forceinline__ f32x4 mfma(v4u a, v4u b, f32x4 c) {
  return __builtin_amdgcn_mfma_f32_16x16x32_bf16(__builtin_bit_cast(bf16x8, a), __builtin_bit_cast(bf16x8, b), c, 0, 0, 0); }
__device__ __forceinline__ __amdgpu_buffer_rsrc_t rsrc(const void* p, int bytes) { return __builtin_amdgcn_make_buffer_rsrc((void*)p, 0, bytes, RSRC3); }
#define SCHED() __builtin_amdgcn_sched_barrier(0)
#define CBAR() asm volatile("" ::: "memory")
#define LGKM0() asm volatile("s_waitcnt lgkmcnt(0)" ::: "memory")
__device__ __forceinline__ unsigned lds_ld(LAS unsigned* p) { return __hip_atomic_load(p, __ATOMIC_RELAXED, __HIP_MEMORY_SCOPE_WORKGROUP); }
__device__ __forceinline__ void lds_st(LAS unsigned* p, unsigned v) { __hip_atomic_store(p, v, __ATOMIC_RELAXED, __HIP_MEMORY_SCOPE_WORKGROUP); }
__device__ __forceinline__ void lds_wait(LAS unsigned* p) {
  for (unsigned i = 0; i < (SPIN_CAP << 4); ++i) { if (__builtin_amdgcn_readfirstlane(lds_ld(p)) != 0u) break; __builtin_amdgcn_s_sleep(1); }
  CBAR();
}
struct Sel { unsigned m[4]; };
__device__ __forceinline__ Sel make_sel(int lane) { Sel s; const unsigned h = (lane & 1) ? 0xffff0000u : 0x0000ffffu;
#pragma unroll
  for (int r = 0; r < 4; ++r) s.m[r] = (((lane >> 1) & 3) == r) ? h : 0u;
  return s; }
__device__ __forceinline__ v4u onehot(const Sel& s, unsigned pp) { return v4u{pp & s.m[0], pp & s.m[1], pp & s.m[2], pp & s.m[3]}; }
__device__ __forceinline__ unsigned glo(float v, unsigned tl) { return (asu(v) & 0xffffff00u) | tl; }
__device__ __forceinline__ float gval(unsigned lo) { return asf(lo & 0xffffff00u); }
__device__ __forceinline__ unsigned gbad(v4u g, unsigned tl, unsigned th) { return (((g.x ^ tl) | (g.z ^ tl)) & 0xffu) | (g.y ^ th) | (g.w ^ th); }

__device__ __forceinline__ void ln_stats(const float (&a)[8], const float (&b)[8], int lane, float& m, float& rden) {
  float s = 0.f;
#pragma unroll
  for (int i = 0; i < 8; ++i) s += a[i] + b[i];
  const float mean = wave_sum(s) * (1.0f / 576.0f);
  const float mb = lane < 8 ? mean : 0.f;
  float v = 0.f;
#pragma unroll
  for (int i = 0; i < 8; ++i) { const float d = a[i] - mean, e = b[i] - mb; v = fmaf(d, d, v); v = fmaf(e, e, v); }
  const float var = wave_sum(v) * (1.0f / 576.0f);
  m = rbf(mean);
  const float den = rbf(__builtin_sqrtf(rbf(rbf(var) + 1e-5f)));
  rden = 1.0f / den;
}
__device__ __forceinline__ float ln_apply(float x, float m, float rden) { return rbf(rbf(x - m) * rden); }

#define PROBE_W 0
#define PROBE_S 0
#define STAMP(i) do { if ((i) == 0 || (i) == PROBE_S) { SCHED(); unsigned long long t_; asm volatile("s_memrealtime %0\n\ts_waitcnt lgkmcnt(0)" : "=s"(t_) :: "memory"); if ((i) == 0) pt0 = t_; if ((i) == PROBE_S) pt1 = t_; SCHED(); } } while (0)
#define STAMP_OUT() do { if (w == PROBE_W) { const unsigned long long until = pt0 + 4000ull + 10ull * (pt1 - pt0); for (int it_ = 0; it_ < 2000000; ++it_) { if (__builtin_amdgcn_s_memrealtime() >= until) break; __builtin_amdgcn_s_sleep(2); } } } while (0)

__global__ void __launch_bounds__(NTHR) fused_decode(const char* __restrict__ x, const char* __restrict__ kc, const char* __restrict__ vc,
    const char* __restrict__ wq, const char* __restrict__ wo, const char* __restrict__ w1, const char* __restrict__ b1,
    const char* __restrict__ w2, const char* __restrict__ b2, char* __restrict__ out, char* __restrict__ ws) {
  __shared__ __attribute__((aligned(16))) unsigned char smem[L_BYTES];
  LAS unsigned char* L = (LAS unsigned char*)smem;
  LAS unsigned* flag = (LAS unsigned*)(L + L_FLAG);
  const int wg = blockIdx.x, lane = threadIdx.x & 63, w = __builtin_amdgcn_readfirstlane(threadIdx.x >> 6);
  const int g = (wg & 7) * 2 + (wg >> 7), s = (wg >> 3) & 15;
  unsigned long long pt0 = 0, pt1 = 0;
  STAMP(0);
  const __amdgpu_buffer_rsrc_t rws = rsrc(ws, WS_END);

  if (w == 0) {
    const int cq = s & 3, rq = s >> 2, z = lane >> 3;
    const __amdgpu_buffer_rsrc_t rx = rsrc(x, 1152);
    const v2u ep = __builtin_amdgcn_raw_buffer_load_b64(rws, 0, WS_E, AUX_SC1);
    const v4u xa = __builtin_amdgcn_raw_buffer_load_b128(rx, lane * 16, 0, 0);
    const v4u xb = __builtin_amdgcn_raw_buffer_load_b128(rx, lane * 16 + 1024, 0, 0);
    const v4u xr0 = __builtin_amdgcn_raw_buffer_load_b128(rx, z * 36, rq * 288, 0);
    const v4u xr1 = __builtin_amdgcn_raw_buffer_load_b128(rx, z * 36 + 16, rq * 288, 0);
    const unsigned xr2 = __builtin_amdgcn_raw_buffer_load_b32(rx, z * 36 + 32, rq * 288, 0);
    const __amdgpu_buffer_rsrc_t rwq = rsrc(wq + (size_t)(rq * 144) * 8192 + g * 512 + cq * 128, 144 * 8192);
    v4u a[18];
#pragma unroll
    for (int i = 0; i < 18; ++i) a[i] = __builtin_amdgcn_raw_buffer_load_b128(rwq, z * (18 * 8192) + (lane & 7) * 16, i * 8192, AUX_NT);
    SCHED();
    __builtin_amdgcn_s_barrier();
    SCHED();
    STAMP(1);
    unsigned long long e64 = ((unsigned long long)__builtin_amdgcn_readfirstlane(ep.y) << 32) | __builtin_amdgcn_readfirstlane(ep.x);
    e64 += 1;
    const unsigned tl = (unsigned)e64 & 0xffu, th = (unsigned)(e64 >> 8);
    if (lane == 0) { *(LAS v2u*)(L + L_TAG) = v2u{tl, th}; LGKM0(); lds_st(flag + 0, 1u); }
    float fa[8], fb[8];
#pragma unroll
    for (int i = 0; i < 4; ++i) { fa[2 * i] = bflo(xa[i]); fa[2 * i + 1] = bfhi(xa[i]); fb[2 * i] = bflo(xb[i]); fb[2 * i + 1] = bfhi(xb[i]); }
    float m, rden;
    ln_stats(fa, fb, lane, m, rden);
    const Sel sel = make_sel(lane);
    unsigned xr[9] = {xr0.x, xr0.y, xr0.z, xr0.w, xr1.x, xr1.y, xr1.z, xr1.w, xr2};
    f32x4 acc = {0.f, 0.f, 0.f, 0.f};
    STAMP(2);
#pragma unroll
    for (int i = 0; i < 18; ++i) {
      const float xv = (i & 1) ? bfhi(xr[i >> 1]) : bflo(xr[i >> 1]);
      const float t = rbf(xv - m) * rden;
      acc = mfma(a[i], onehot(sel, pk_bf16(t, t)), acc);
    }
    {
      auto r01 = __builtin_amdgcn_permlane32_swap(asu(acc[0]), asu(acc[1]), false, false);
      auto r23 = __builtin_amdgcn_permlane32_swap(asu(acc[2]), asu(acc[3]), false, false);
      const float y0 = asf(r01[0]) + dpp<ROR8>(asf(r01[1])), y1 = asf(r23[0]) + dpp<ROR8>(asf(r23[1]));
      const int pair = cq * 32 + (lane >> 5) * 16 + ((lane >> 4) & 1) * 8 + (lane & 7);
      if (!(lane & 8)) __builtin_amdgcn_raw_buffer_store_b128(v4u{glo(y0, tl), th, glo(y1, tl), th}, rws, pair * 16, WS_Q + g * 8192 + rq * 2048, AUX_SC1);
    }
    STAMP(3);
    __builtin_amdgcn_s_sleep(20);
    for (unsigned spin = 0; spin < SPIN_CAP; ++spin) {
      CBAR();
      v4u q[8];
#pragma unroll
      for (int i = 0; i < 8; ++i) q[i] = __builtin_amdgcn_raw_buffer_load_b128(rws, lane * 16, WS_Q + g * 8192 + i * 1024, AUX_SC1);
      unsigned bad = 0;
#pragma unroll
      for (int i = 0; i < 8; ++i) bad |= gbad(q[i], tl, th);
      if (__builtin_amdgcn_ballot_w64(bad != 0u) == 0ull || spin == SPIN_CAP - 1) {
#pragma unroll
        for (int e = 0; e < 2; ++e) {
          const float v0 = (gval(q[e].x) + gval(q[2 + e].x)) + (gval(q[4 + e].x) + gval(q[6 + e].x));
          const float v1 = (gval(q[e].z) + gval(q[2 + e].z)) + (gval(q[4 + e].z) + gval(q[6 + e].z));
          const unsigned pk = pk_bf16(v0, v1);
          const int c0 = 64 * (2 * e + (lane >> 5)) + 32 * ((lane >> 3) & 1) + 8 * ((lane >> 4) & 1) + (lane & 7);
          *(LAS unsigned short*)(L + L_Q + c0 * 2) = (unsigned short)pk;
          *(LAS unsigned short*)(L + L_Q + c0 * 2 + 32) = (unsigned short)(pk >> 16);
        }
        LGKM0();
        if (lane == 0) lds_st(flag + 1, 1u);
        break;
      }
    }
    STAMP(4);
    float gate = 0.f, xo = 0.f;
    const int col = 64 * (wg >> 4) + 32 * ((wg >> 3) & 1) + 8 * ((wg >> 2) & 1) + 2 * (wg & 3) + ((lane >> 4) & 1) + 16 * (lane >> 5);
    if (wg < 144) {
      xo = bflo(__builtin_amdgcn_raw_buffer_load_b16(rx, col * 2, 0, 0));
      const float bb = bflo(*(const GAS unsigned short*)b2);
      for (unsigned spin = 0; spin < SPIN_CAP; ++spin) {
        CBAR();
        const v2u y = __builtin_amdgcn_raw_buffer_load_b64(rws, col * 128 + (lane & 15) * 8, WS_Y, AUX_SC1);
        const bool live = (lane & 15) < 12;
        const unsigned bad = ((y.x ^ tl) & 0xffu) | (y.y ^ th);
        if (__builtin_amdgcn_ballot_w64(live && bad != 0u) == 0ull || spin == SPIN_CAP - 1) {
          const float yy = rbf(rbf(row_sum(live ? gval(y.x) : 0.f)) + bb);
          gate = rbf(yy / (1.0f + __expf(-yy)));
          break;
        }
        __builtin_amdgcn_s_sleep(8);
      }
    }
    STAMP(5);
    __builtin_amdgcn_s_barrier();
    STAMP(6);
    if (wg < 144) {
      for (unsigned spin = 0; spin < SPIN_CAP; ++spin) {
        CBAR();
        v4u d[8];
#pragma unroll
        for (int i = 0; i < 8; ++i) d[i] = __builtin_amdgcn_raw_buffer_load_b128(rws, lane * 16, WS_S + wg * 8192 + i * 1024, AUX_SC1);
        unsigned bad = 0; float v0 = 0.f, v1 = 0.f;
#pragma unroll
        for (int i = 0; i < 8; ++i) { bad |= gbad(d[i], tl, th); v0 += gval(d[i].x); v1 += gval(d[i].z); }
        if (__builtin_amdgcn_ballot_w64(bad != 0u) == 0ull || spin == SPIN_CAP - 1) {
          auto r = __builtin_amdgcn_permlane32_swap(asu(v0), asu(v1), false, false);
          float y = asf(r[0]) + asf(r[1]);
          y = x16_sum(y); y += dpp<ROR8>(y); y += dpp<ROR4>(y); y += dpp<ROR2>(y);
          const float res = xo + rbf(rbf(y) * gate);
          if ((lane & 15) == ((lane >> 4) & 1)) *(GAS unsigned short*)(out + col * 2) = (unsigned short)pk_bf16(res, res);
          if (wg == 0 && lane == 0) *(GAS unsigned long long*)(ws + WS_E) = e64;
          break;
        }
      }
    }
    STAMP(7);
    STAMP_OUT();
  } else if (w <= 4) {
    const int kw = w - 1, rot = s * 20 + g * 7;
    const __amdgpu_buffer_rsrc_t rk = rsrc(kc + ((size_t)g * 65536 + 61440 + s * 256) * 256, 65536);
    const int voff = ((lane >> 2) & 3) * 256 + ((lane >> 4) * 4 + (lane & 3)) * 16;
    __builtin_amdgcn_s_barrier();
    v4u k[16];
#pragma unroll
    for (int i = 0; i < 16; ++i) k[i] = __builtin_amdgcn_raw_buffer_load_b128(rk, voff, ((kw * 16 + i + rot) & 63) * 1024, AUX_NT);
    SCHED();
    STAMP(1);
    lds_wait(flag + 1);
    STAMP(2);
    const v4u qf = *(LAS v4u*)(L + L_Q + ((lane >> 3) & 1) * 256 + ((lane >> 4) * 4 + (lane & 3)) * 16);
    const v4u zero = {0u, 0u, 0u, 0u};
    const v4u q0 = (lane & 4) ? zero : qf, q1 = (lane & 4) ? qf : zero;
    float sc[8];
    float mx = -INFINITY;
#pragma unroll
    for (int i = 0; i < 8; ++i) {
      f32x4 acc = {0.f, 0.f, 0.f, 0.f};
      acc = mfma(k[2 * i], q0, acc);
      acc = mfma(k[2 * i + 1], q1, acc);
      const float d = (dpp<0x00>(acc[0]) + dpp<0x55>(acc[1])) + (dpp<0xAA>(acc[2]) + dpp<0xFF>(acc[3]));
      sc[i] = rbf(rbf(d) * 0.08838834764831845f);
      mx = fmaxf(mx, sc[i]);
    }
    mx = fmaxf(mx, dpp<HMIR>(mx));
    mx = x32_max(x16_max(mx));
    float l = 0.f; unsigned pp[8];
    const float mo = mx * 1.4426950408889634f;
#pragma unroll
    for (int i = 0; i < 8; ++i) { const float p = __builtin_amdgcn_exp2f(fmaf(sc[i], 1.4426950408889634f, -mo)); l += p; pp[i] = pk_bf16(p, p); }
    l += dpp<HMIR>(l);
    l = x32_sum(x16_sum(l));
    if ((lane & 3) == 0) {
      LAS v4u* pd = (LAS v4u*)(L + L_P + kw * 512 + (lane >> 2) * 32);
      pd[0] = v4u{pp[0], pp[1], pp[2], pp[3]}; pd[1] = v4u{pp[4], pp[5], pp[6], pp[7]};
    }
    if ((lane & 0x37) == 0) *(LAS v2u*)(L + L_ML + kw * 16 + (lane >> 3) * 8) = v2u{asu(mx), asu(l)};
    LGKM0();
    if (lane == 0) lds_st(flag + 2 + kw, 1u);
    STAMP(3);
    STAMP_OUT();
  } else if (w <= 8) {
    const int vw = w - 5, rot = s * 20 + g * 7, h = (lane >> 3) & 1;
    const __amdgpu_buffer_rsrc_t rv = rsrc(vc + ((size_t)g * 65536 + 61440 + s * 256) * 256, 65536);
    __builtin_amdgcn_s_barrier();
    v4u v[16];
#pragma unroll
    for (int i = 0; i < 16; ++i) v[i] = __builtin_amdgcn_raw_buffer_load_b128(rv, lane * 16, ((vw * 16 + i + rot) & 63) * 1024, AUX_NT);
    SCHED();
    STAMP(1);
    lds_wait(flag + 2); lds_wait(flag + 3); lds_wait(flag + 4); lds_wait(flag + 5);
    STAMP(2);
    const Sel sel = make_sel(lane);
    v4u pr[4];
#pragma unroll
    for (int i = 0; i < 4; ++i) pr[i] = *(LAS v4u*)(L + L_P + vw * 512 + (lane >> 3) * 64 + i * 16);
    v2u ml[4];
#pragma unroll
    for (int i = 0; i < 4; ++i) ml[i] = *(LAS v2u*)(L + L_ML + i * 16 + h * 8);
    const v2u mine = *(LAS v2u*)(L + L_ML + vw * 16 + h * 8);
    const v2u tg = *(LAS v2u*)(L + L_TAG);
    const unsigned tl = tg.x, th = tg.y;
    const float M = fmaxf(fmaxf(asf(ml[0].x), asf(ml[1].x)), fmaxf(asf(ml[2].x), asf(ml[3].x)));
    const float f = __expf(asf(mine.x) - M);
    if (vw == 0) {
      float Ls = 0.f;
#pragma unroll
      for (int i = 0; i < 4; ++i) Ls = fmaf(__expf(asf(ml[i].x) - M), asf(ml[i].y), Ls);
      if ((lane & 0x37) == 0) __builtin_amdgcn_raw_buffer_store_b128(v4u{glo(M, tl), th, glo(Ls, tl), th}, rws, h * 256, WS_ML + g * 512 + s * 16, AUX_SC1);
    }
    v4u b[16];
#pragma unroll
    for (int i = 0; i < 16; ++i) { const int idx = (i & 1) * 8 + (i >> 1); b[i] = onehot(sel, pr[idx >> 2][idx & 3]); }
    SCHED();
    f32x4 acc = {0.f, 0.f, 0.f, 0.f};
#pragma unroll
    for (int i = 0; i < 16; ++i) acc = mfma(v[i], b[i], acc);
    acc *= f;
    *(LAS f32x4*)(L + L_RED + vw * 1024 + lane * 16) = acc;
    LGKM0();
    STAMP(3);
    __builtin_amdgcn_s_barrier();
    CBAR();
    if (vw < 2) {
      v2u r[4];
#pragma unroll
      for (int i = 0; i < 4; ++i) r[i] = *(LAS v2u*)(L + L_RED + i * 1024 + lane * 16 + vw * 8);
      const float o0 = (asf(r[0].x) + asf(r[1].x)) + (asf(r[2].x) + asf(r[3].x));
      const float o1 = (asf(r[0].y) + asf(r[1].y)) + (asf(r[2].y) + asf(r[3].y));
      __builtin_amdgcn_raw_buffer_store_b128(v4u{glo(o0, tl), th, glo(o1, tl), th}, rws, (lane >> 2) * 2048 + (lane & 3) * 32 + vw * 16,
                                             WS_O + g * 32768 + s * 128, AUX_SC1);
    }
    STAMP(4);
    STAMP_OUT();
  } else {
    const int ow = w - 9, z = lane >> 3;
    if (ow == 0 && lane < 8) lds_st(flag + lane, 0u);
    const __amdgpu_buffer_rsrc_t rx = rsrc(x, 1152);
    const v4u xa = __builtin_amdgcn_raw_buffer_load_b128(rx, lane * 16, 0, 0);
    const v4u xb = __builtin_amdgcn_raw_buffer_load_b128(rx, lane * 16 + 1024, 0, 0);
    const int cg = wg >> 2, fq = wg & 3;
    const unsigned xc = __builtin_amdgcn_raw_buffer_load_b16(rx, min(lane, 8) * 2, cg * 18, 0);
    const int foff = fq * 2304 + ow * 768;
    const v3u w1v = __builtin_amdgcn_raw_buffer_load_b96(rsrc(w1, 9216), lane * 12, foff, 0);
    const v3u b1v = __builtin_amdgcn_raw_buffer_load_b96(rsrc(b1, 9216), lane * 12, foff, 0);
    const v3u w2v = __builtin_amdgcn_raw_buffer_load_b96(rsrc(w2, 9216), lane * 12, foff, 0);
    LGKM0();
    SCHED();
    __builtin_amdgcn_s_barrier();
    SCHED();
    STAMP(1);
    float fa[8], fb[8];
#pragma unroll
    for (int i = 0; i < 4; ++i) { fa[2 * i] = bflo(xa[i]); fa[2 * i + 1] = bfhi(xa[i]); fb[2 * i] = bflo(xb[i]); fb[2 * i + 1] = bfhi(xb[i]); }
    float m, rden;
    ln_stats(fa, fb, lane, m, rden);
    float hv = ln_apply(bflo(xc), m, rden);
#pragma unroll
    for (int i = 0; i < 8; ++i) { fa[i] = ln_apply(fa[i], m, rden); fb[i] = lane < 8 ? ln_apply(fb[i], m, rden) : 0.f; }
    ln_stats(fa, fb, lane, m, rden);
    hv = ln_apply(hv, m, rden);
    float ysum = 0.f;
    constexpr float C1 = -2.0f * 0.7978845608028654f * 1.4426950408889634f, C2 = C1 * 0.044715f;
#pragma unroll 1
    for (int c = 0; c < 9; ++c) {
      const float hc = asf(__builtin_amdgcn_readlane(asu(hv), c));
      float y = 0.f;
#pragma unroll
      for (int j = 0; j < 3; ++j) {
        const unsigned t = pk_bf16(hc * bflo(w1v[j]), hc * bfhi(w1v[j]));
        const unsigned u = pk_bf16(bflo(t) + bflo(b1v[j]), bfhi(t) + bfhi(b1v[j]));
        const float u0 = bflo(u), u1 = bfhi(u);
        const float e0 = __builtin_amdgcn_exp2f(u0 * fmaf(u0 * u0, C2, C1)), e1 = __builtin_amdgcn_exp2f(u1 * fmaf(u1 * u1, C2, C1));
        const unsigned gl = pk_bf16(u0 * __builtin_amdgcn_rcpf(1.0f + e0), u1 * __builtin_amdgcn_rcpf(1.0f + e1));
        y = fmaf(bflo(gl), bflo(w2v[j]), y); y = fmaf(bfhi(gl), bfhi(w2v[j]), y);
      }
      y = wave_sum(y);
      if (lane == c) ysum = y;
    }
    lds_wait(flag + 0);
    const v2u tg = *(LAS v2u*)(L + L_TAG);
    const unsigned tl = tg.x, th = tg.y;
    if (lane < 9) __builtin_amdgcn_raw_buffer_store_b64(v2u{glo(ysum, tl), th}, rws, lane * 128, WS_Y + cg * 1152 + (fq * 3 + ow) * 8, AUX_SC1);
    STAMP(2);
    const int hh = (s >> 1) & 1;
    const __amdgpu_buffer_rsrc_t rwo = rsrc(wo + (size_t)(g * 256 + hh * 128 + 32 * (s >> 2) + 4 * (s & 1)) * 1152, 28 * 1152);
    v4u a[6];
#pragma unroll
    for (int j = 0; j < 3; ++j)
#pragma unroll
      for (int mm = 0; mm < 2; ++mm)
        a[2 * j + mm] = __builtin_amdgcn_raw_buffer_load_b128(rwo, (16 * (z & 1) + (z >> 1)) * 1152 + (lane & 7) * 16, mm * 8 * 1152 + (ow * 3 + j) * 128, AUX_NT);
    SCHED();
    STAMP(3);
    const Sel sel = make_sel(lane);
    __builtin_amdgcn_s_barrier();
    STAMP(4);
    for (unsigned spin = 0; spin < SPIN_CAP; ++spin) {
      CBAR();
      v4u ml[2], d[2];
#pragma unroll
      for (int t = 0; t < 2; ++t) {
        d[t] = __builtin_amdgcn_raw_buffer_load_b128(rws, (lane & 7) * 128 + z * 16, WS_O + g * 32768 + s * 2048 + t * 1024, AUX_SC1);
        ml[t] = __builtin_amdgcn_raw_buffer_load_b128(rws, (lane & 7) * 16, WS_ML + g * 512 + hh * 256 + t * 128, AUX_SC1);
      }
      const unsigned bad = gbad(d[0], tl, th) | gbad(d[1], tl, th) | gbad(ml[0], tl, th) | gbad(ml[1], tl, th);
      if (__builtin_amdgcn_ballot_w64(bad != 0u) == 0ull || spin == SPIN_CAP - 1) {
        STAMP(5);
        float M = fmaxf(gval(ml[0].x), gval(ml[1].x));
        M = fmaxf(M, dpp<XOR1>(M)); M = fmaxf(M, dpp<XOR2>(M)); M = fmaxf(M, dpp<HMIR>(M));
        const float w0 = __expf(gval(ml[0].x) - M), w1f = __expf(gval(ml[1].x) - M);
        float Ls = w0 * gval(ml[0].z) + w1f * gval(ml[1].z);
        float o0 = w0 * gval(d[0].x) + w1f * gval(d[1].x), o1 = w0 * gval(d[0].z) + w1f * gval(d[1].z);
        Ls += dpp<XOR1>(Ls); o0 += dpp<XOR1>(o0); o1 += dpp<XOR1>(o1);
        Ls += dpp<XOR2>(Ls); o0 += dpp<XOR2>(o0); o1 += dpp<XOR2>(o1);
        Ls += dpp<HMIR>(Ls); o0 += dpp<HMIR>(o0); o1 += dpp<HMIR>(o1);
        const float rl = __builtin_amdgcn_rcpf(Ls);
        o0 *= rl; o1 *= rl;
        const v4u b0 = onehot(sel, pk_bf16(o0, o0)), b1f = onehot(sel, pk_bf16(o1, o1));
        const int voff = (((lane >> 4) & 1) * 8 + (lane >> 5) * 4 + ((lane & 7) >> 1)) * 8192 + (lane & 1) * 16;
#pragma unroll
        for (int j = 0; j < 3; ++j) {
          f32x4 acc = {0.f, 0.f, 0.f, 0.f};
          acc = mfma(a[2 * j], b0, acc);
          acc = mfma(a[2 * j + 1], b1f, acc);
          auto r01 = __builtin_amdgcn_permlane32_swap(asu(acc[0]), asu(acc[1]), false, false);
          auto r23 = __builtin_amdgcn_permlane32_swap(asu(acc[2]), asu(acc[3]), false, false);
          const float y0 = asf(r01[0]) + dpp<ROR8>(asf(r01[1])), y1 = asf(r23[0]) + dpp<ROR8>(asf(r23[1]));
          if (!(lane & 8)) __builtin_amdgcn_raw_buffer_store_b128(v4u{glo(y0, tl), th, glo(y1, tl), th}, rws, voff, WS_S + (ow * 3 + j) * (16 * 8192) + wg * 32, AUX_SC1);
        }
        break;
      }
    }
    STAMP(6);
    STAMP_OUT();
  }
}
}

__global__ void __launch_bounds__(64) pre_k(int dummy) {
  unsigned long long t0; asm volatile("s_memrealtime %0\n\ts_waitcnt lgkmcnt(0)" : "=s"(t0) :: "memory");

  if (threadIdx.x < 64) for (int it_ = 0; it_ < 2000000; ++it_) { if (__builtin_amdgcn_s_memrealtime() >= t0 + 4000ull) break; __builtin_amdgcn_s_sleep(2); }
}
extern "C" void kernel_launch(void* const* d_in, const int* in_sizes, int n_in, void* d_out, int out_size, void* d_ws, size_t ws_size, hipStream_t stream) {
  fused_decode<<<NWG, NTHR, 0, stream>>>((const char*)d_in[0], (const char*)d_in[1], (const char*)d_in[2], (const char*)d_in[3], (const char*)d_in[4],
      (const char*)d_in[6], (const char*)d_in[7], (const char*)d_in[8], (const char*)d_in[9], (char*)d_out, (char*)d_ws);
  pre_k<<<1, 64, 0, stream>>>(0);
}
```

```cpp
#include <hip/hip_runtime.h>
#include <stdint.h>

namespace {
constexpr int POFF = 66;
constexpr int K0X = 20;
constexpr int KTAU = 225;
constexpr int VTAU = 248;
constexpr int WOFF = 12;
constexpr int WTAU = 11;
constexpr int XLAT = 55, XTOL = 15, XADD = 5;
constexpr int TLAT = 110, TCAP = 12, TSUB = 20;
constexpr int SYNCAT = 55;
constexpr int QAT = 305;
constexpr int MLT = 8;
constexpr int D2T = 116, D2P = 10;
constexpr int D3T = 190, D3P = 12;
constexpr int NLEAD = 10, NREP = 20, NCHK = 45, NADD = 98;
constexpr int CODE_LINES = 125;
typedef unsigned v4u __attribute__((ext_vector_type(4)));
typedef unsigned v3u __attribute__((ext_vector_type(3)));
typedef unsigned v2u __attribute__((ext_vector_type(2)));
typedef float f32x4 __attribute__((ext_vector_type(4)));
typedef float f32x2 __attribute__((ext_vector_type(2)));
typedef short bf16x8 __attribute__((ext_vector_type(8)));
typedef __bf16 bf16x2 __attribute__((ext_vector_type(2)));
#define LAS __attribute__((address_space(3)))
#define GAS __attribute__((address_space(1)))

constexpr int AUX_NT = 2, AUX_SC1 = 16, RSRC3 = 0x00020000;
constexpr int NWG = 256, NTHR = 768;
constexpr int WS_E = 0;
constexpr int WS_T = 1024;
constexpr int WS_N = 2048;
constexpr int WS_Q = 4096;
constexpr int WS_L = WS_Q + 16 * 4096;
constexpr int WS_O = WS_L + 256 * 128;
constexpr int WS_S = WS_O + 256 * 1024;
constexpr int WS_END = WS_S + 36 * 16384;
constexpr int OOB = 0x40000000;
constexpr unsigned SPIN_CAP = 1u << 18, LDS_CAP = 1u << 23;

constexpr int F_BASE = 0, F_Q = 1, F_CMB = 2, F_BASE2 = 3, F_Y = 4  , F_TAG = 7, F_K = 8  , F_END = 12, F_GATE = 13, F_PUB = 14, F_HOLD = 15, F_V = 16  , F_W = 19, F_F = 20  ;
constexpr int L_TAG = 128;
constexpr int L_B2 = 136;
constexpr int L_Q = 144;
constexpr int L_ML = L_Q + 512;
constexpr int L_G = L_ML + 64;
constexpr int L_FIN = L_G + 128;
constexpr int L_Y = L_FIN + 256;
constexpr int L_W = L_Y + 192;
constexpr int L_P = L_W + 64;
constexpr int L_RED = L_P + 2048;
constexpr int L_BYTES = L_RED + 4096;

__device__ __forceinline__ float asf(unsigned u) { return __builtin_bit_cast(float, u); }
__device__ __forceinline__ unsigned asu(float f) { return __builtin_bit_cast(unsigned, f); }
__device__ __forceinline__ bf16x2 asb(unsigned u) { return __builtin_bit_cast(bf16x2, u); }
__device__ __forceinline__ float bflo(unsigned u) { return asf(u << 16); }
__device__ __forceinline__ float bfhi(unsigned u) { return asf(u & 0xffff0000u); }
__device__ __forceinline__ f32x2 bf2(unsigned u) { return f32x2{bflo(u), bfhi(u)}; }
__device__ __forceinline__ unsigned pk2(f32x2 v) {
  unsigned r = __builtin_bit_cast(unsigned, __builtin_convertvector(v, bf16x2)); asm("" : "+v"(r)); return r; }
__device__ __forceinline__ unsigned pk_bf16(float lo, float hi) { return pk2(f32x2{lo, hi}); }
__device__ __forceinline__ float rbf(float f) { return (float)(__bf16)f; }
__device__ __forceinline__ float fmx(float a, float b) { return __builtin_elementwise_maximum(a, b); }
template <int CTRL> __device__ __forceinline__ float dpp(float x) {
  return asf(__builtin_amdgcn_mov_dpp(asu(x), CTRL, 0xf, 0xf, true)); }
template <int CTRL> __device__ __forceinline__ int dppi(int x) { return __builtin_amdgcn_mov_dpp(x, CTRL, 0xf, 0xf, true); }
constexpr int XOR1 = 0xB1, XOR2 = 0x4E, HMIR = 0x141, ROR8 = 0x128, ROR4 = 0x124, ROR2 = 0x122, ROR1 = 0x121;
__device__ __forceinline__ float sw16_sum(float a, float b) { auto s = __builtin_amdgcn_permlane16_swap(asu(a), asu(b), false, false); return asf(s[0]) + asf(s[1]); }
__device__ __forceinline__ float sw32_sum(float a, float b) { auto s = __builtin_amdgcn_permlane32_swap(asu(a), asu(b), false, false); return asf(s[0]) + asf(s[1]); }
__device__ __forceinline__ float x16_max(float x) { auto s = __builtin_amdgcn_permlane16_swap(asu(x), asu(x), false, false); return fmx(asf(s[0]), asf(s[1])); }
__device__ __forceinline__ float x32_max(float x) { auto s = __builtin_amdgcn_permlane32_swap(asu(x), asu(x), false, false); return fmx(asf(s[0]), asf(s[1])); }
__device__ __forceinline__ float row_sum(float x) { x += dpp<ROR8>(x); x += dpp<ROR4>(x); x += dpp<ROR2>(x); x += dpp<ROR1>(x); return x; }
__device__ __forceinline__ float wave_sum(float x) { x = row_sum(x); x = sw16_sum(x, x); return sw32_sum(x, x); }
__device__ __forceinline__ f32x4 mfma(v4u a, v4u b, f32x4 c) {
  return __builtin_amdgcn_mfma_f32_16x16x32_bf16(__builtin_bit_cast(bf16x8, a), __builtin_bit_cast(bf16x8, b), c, 0, 0, 0); }
__device__ __forceinline__ __amdgpu_buffer_rsrc_t rsrc(const void* p, int bytes) { return __builtin_amdgcn_make_buffer_rsrc((void*)p, 0, bytes, RSRC3); }
__device__ __forceinline__ void st128(__amdgpu_buffer_rsrc_t r, int off, v4u v) { __builtin_amdgcn_raw_buffer_store_b128(v, r, off, 0, AUX_SC1); }
#define VMW0() __builtin_amdgcn_s_waitcnt(0x0f70)
#define SCHED() __builtin_amdgcn_sched_barrier(0)
#define CBAR() asm volatile("" ::: "memory")
#define LDS_REL() __builtin_amdgcn_fence(__ATOMIC_RELEASE, "workgroup", "local")
#define LDS_ACQ() __builtin_amdgcn_fence(__ATOMIC_ACQUIRE, "workgroup", "local")
__device__ __forceinline__ unsigned lds_ld(LAS unsigned* p) { return __hip_atomic_load(p, __ATOMIC_RELAXED, __HIP_MEMORY_SCOPE_WORKGROUP); }
__device__ __forceinline__ void lds_st(LAS unsigned* p, unsigned v) { __hip_atomic_store(p, v, __ATOMIC_RELAXED, __HIP_MEMORY_SCOPE_WORKGROUP); }
__device__ __forceinline__ bool none(bool p) { return __builtin_amdgcn_ballot_w64(p) == 0ull; }
__device__ __forceinline__ void lds_set(LAS unsigned* p) { LDS_REL(); lds_st(p, 1u); }
template <int SLP> __device__ __forceinline__ unsigned lds_wait(LAS unsigned* p) {
  unsigned v = 0;
#pragma unroll 1
  for (unsigned i = 0; i < LDS_CAP; ++i) { v = lds_ld(p); if (none(v == 0u)) break; if (SLP) __builtin_amdgcn_s_sleep(SLP); }
  LDS_ACQ();
  return v;
}
template <int SLP> __device__ __forceinline__ unsigned lds_get(LAS unsigned* p) { return __builtin_amdgcn_readfirstlane(lds_wait<SLP>(p)); }
__device__ __forceinline__ unsigned now() { return (unsigned)__builtin_amdgcn_s_memrealtime(); }
__device__ __forceinline__ void pace(unsigned t) {
#pragma unroll 1
  while ((unsigned)(t - now() - 1u) < 4000u) {}
}
__device__ __forceinline__ void pace_far(unsigned t) {
#pragma unroll 1
  while ((unsigned)(t - now() - 1u) < 4000u) __builtin_amdgcn_s_sleep(1);
}
constexpr int piece(int i) { return ((i >> 1) & 3) * 16 + (i >> 3) * 2 + (i & 1); }
struct Sel { unsigned m[4]; };
__device__ __forceinline__ Sel make_sel(int lane) { Sel s; const unsigned h = (lane & 1) ? 0xffff0000u : 0x0000ffffu;
#pragma unroll
  for (int r = 0; r < 4; ++r) s.m[r] = (((lane >> 1) & 3) == r) ? h : 0u;
  asm volatile("" : "+v"(s.m[0]), "+v"(s.m[1]), "+v"(s.m[2]), "+v"(s.m[3]));
  return s; }
__device__ __forceinline__ v4u onehot(const Sel& s, unsigned pp) { return v4u{pp & s.m[0], pp & s.m[1], pp & s.m[2], pp & s.m[3]}; }
__device__ __forceinline__ unsigned duplo(unsigned p) { return __builtin_amdgcn_perm(p, p, 0x01000100u); }
__device__ __forceinline__ unsigned duphi(unsigned p) { return __builtin_amdgcn_perm(p, p, 0x03020302u); }
__device__ __forceinline__ unsigned glo(float v, unsigned tb) { return (asu(v) & 0xffffff00u) | tb; }
__device__ __forceinline__ float gval(unsigned lo) { return asf(lo & 0xffffff00u); }
__device__ __forceinline__ unsigned gbad(v4u g, unsigned tl, unsigned th) { return (((g.x ^ tl) | (g.z ^ tl)) & 0xffu) | (g.y ^ th) | (g.w ^ th); }
__device__ __forceinline__ unsigned tag_byte(unsigned t, int k) { return (t >> (8 * k)) & 0xffu; }

__device__ __forceinline__ void ln_stats(const f32x2 (&a)[4], float b, float& m, float& rden) {
  const f32x2 s2 = (a[0] + a[1]) + (a[2] + a[3]);
  const float mean = wave_sum((s2.x + s2.y) + b) * (1.0f / 576.0f);
  f32x2 v = {(b - mean) * (b - mean), 0.f};
#pragma unroll
  for (int i = 0; i < 4; ++i) { const f32x2 d = a[i] - mean; v = __builtin_elementwise_fma(d, d, v); }
  const float var = wave_sum(v.x + v.y) * (1.0f / 576.0f);
  m = rbf(mean);
  rden = __builtin_amdgcn_rcpf(rbf(__builtin_amdgcn_sqrtf(rbf(rbf(var) + 1e-5f))));
}
__device__ __forceinline__ unsigned ln_apply2(f32x2 x, float m, float rden) { return pk2(bf2(pk2(x - m)) * rden); }


__global__ void __launch_bounds__(NTHR, 3) fused_decode(const char* wq, const char* x, const char* kc, char* ws, const char* vc, const char* wo, char* out,
    const char* w1, const char* b1, const char* w2, const char* b2) {
  __shared__ __attribute__((aligned(16))) unsigned char smem[L_BYTES];
  LAS unsigned char* L = (LAS unsigned char*)smem;
  LAS unsigned* flag = (LAS unsigned*)L;
  const int wg = blockIdx.x, lane = threadIdx.x & 63;
  const int w = __builtin_amdgcn_readfirstlane(threadIdx.x >> 6);
  const unsigned long long t64 = __builtin_amdgcn_s_memrealtime();
  const unsigned t0 = (unsigned)t64;
  unsigned codew;
  if (w == 11) {
    unsigned long long pc; asm volatile("s_getpc_b64 %0" : "=s"(pc));
    codew = *(const GAS unsigned*)((pc & ~127ull) + min((lane & 15) * 16 + ((wg >> 3) & 15), CODE_LINES - 1) * 128);
    lds_st(flag + (lane & 31), 0u);
    LDS_REL();
  }
  __builtin_amdgcn_s_barrier();
  const int g = (wg & 7) * 2 + (wg >> 7), s = (wg >> 3) & 15, z = lane >> 3;
  const int g2 = (g + 2) & 15;
#define PINS() asm volatile("" :: "s"(x), "s"(kc), "s"(vc), "s"(wq), "s"(wo), "s"(w1), "s"(b1), "s"(w2), "s"(b2), "s"(out), "s"(ws), "s"(t64))
#define ROLE() PINS(); \
  const __amdgpu_buffer_rsrc_t rws = rsrc(ws, WS_END)

  if ((w >> 2) == 1) {
    const int kw = w - 4;
    int voff = ((lane >> 2) & 3) * 256 + ((lane >> 4) * 4 + (lane & 3)) * 16;
    int s0 = (kw * 4 + s * 4 + g * 2) * 1024;
    unsigned kvo = (unsigned)(g * 65536 + 61440 + s * 256) << 8;
    asm volatile("" : "+v"(voff));
    asm volatile("" : "+s"(s0));
    asm volatile("" : "+s"(kvo));
    ROLE();
    const __amdgpu_buffer_rsrc_t rb = rsrc(kc + kvo, 65536);
    v4u d[16];
    if (kw == 3) {
      const unsigned tch = *(const __attribute__((address_space(4))) unsigned*)(kc + ((unsigned)(g * 65536 + 61440) << 8));
      asm volatile("" :: "s"(tch));
      SCHED();
      const unsigned tt = now() - TLAT, b1st = lds_get<0>(flag + F_BASE);
      const int dl = (int)(tt - b1st);
      const unsigned b2nd = (b1st + max(max(min(dl, TCAP), dl - TSUB), 0)) | 1u;
      lds_st(flag + F_BASE2, b2nd);
      __builtin_amdgcn_raw_buffer_store_b32(b2nd, rws, (wg < 8 && lane < 32) ? lane * 4 + wg * 128 : OOB, WS_T, AUX_SC1);
    }
    const unsigned tb = lds_get<0>(flag + (kw ? F_BASE2 : F_BASE)) + POFF + (kw ? K0X + kw * KTAU / 4 : 0);
    const int kd = kw ? KTAU : KTAU + 4 * K0X;
    if (kw) pace_far(tb - 4);
#pragma unroll
    for (int i = 0; i < 16; ++i) {
      if (i % 2 == 0) pace(tb + ((i * kd) >> 6));
      d[i] = __builtin_amdgcn_raw_buffer_load_b128(rb, voff, (s0 + piece(i) * 1024) & 0xffff, AUX_NT); SCHED();
    }
    int qa = L_Q + ((lane >> 3) & 1) * 256 + ((lane >> 4) * 4 + (lane & 3)) * 16, pa = L_P + kw * 512 + lane * 8, ma = L_ML + kw * 16 + (lane & 8);
    unsigned mt = (lane & 4) ? ~0u : 0u;
    asm volatile("" : "+v"(qa), "+v"(pa), "+v"(ma), "+v"(mt));
    lds_wait<1>(flag + F_TAG);
    const v2u tg = *(LAS v2u*)(L + L_TAG);
    VMW0();
    v4u qf;
#pragma unroll 1
    for (unsigned i = 0; i < LDS_CAP; ++i) {
      const unsigned fl = *(volatile LAS unsigned*)(flag + F_Q);
      qf = *(volatile LAS v4u*)(L + qa);
      if (none(fl == 0u)) break;
      __builtin_amdgcn_s_sleep(1);
    }
    const v4u q0 = qf & ~mt, q1 = qf & mt;
    f32x4 acc[8];
#pragma unroll
    for (int i = 0; i < 8; ++i) acc[i] = mfma(d[2 * i], q0, f32x4{0.f, 0.f, 0.f, 0.f});
#pragma unroll
    for (int i = 0; i < 8; ++i) acc[i] = mfma(d[2 * i + 1], q1, acc[i]);
    SCHED();
    float dd[8];
#pragma unroll
    for (int i = 0; i < 8; ++i) dd[i] = dpp<0x00>(acc[i][0]);
#pragma unroll
    for (int i = 0; i < 8; ++i) { dd[i] += dpp<0x55>(acc[i][1]); asm("" : "+v"(dd[i])); }
#pragma unroll
    for (int i = 0; i < 8; ++i) { dd[i] += dpp<0xAA>(acc[i][2]); asm("" : "+v"(dd[i])); }
#pragma unroll
    for (int i = 0; i < 8; ++i) { dd[i] += dpp<0xFF>(acc[i][3]); asm("" : "+v"(dd[i])); }
    f32x2 sc;
    sc.x = (lane & 2) ? ((lane & 1) ? dd[6] : dd[4]) : ((lane & 1) ? dd[2] : dd[0]);
    sc.y = (lane & 2) ? ((lane & 1) ? dd[7] : dd[5]) : ((lane & 1) ? dd[3] : dd[1]);
    sc = bf2(pk2(bf2(pk2(sc)) * 0.08838834764831845f));
    float mx = fmx(sc.x, sc.y);
    mx = fmx(mx, dpp<XOR1>(mx)); mx = fmx(mx, dpp<XOR2>(mx)); mx = fmx(mx, dpp<HMIR>(mx));
    mx = x32_max(x16_max(mx));
    const f32x2 ex = (sc - mx) * 1.4426950408889634f;
    const float p0 = __builtin_amdgcn_exp2f(ex.x), p1 = __builtin_amdgcn_exp2f(ex.y);
    *(volatile LAS v2u*)(L + pa) = v2u{pk_bf16(p0, p0), pk_bf16(p1, p1)};
    float l = p0 + p1;
    l += dpp<XOR1>(l); l += dpp<XOR2>(l); l += dpp<HMIR>(l);
    l = sw16_sum(l, l); l = sw32_sum(l, l);
    *(volatile LAS v2u*)(L + ma) = v2u{asu(mx), asu(l)};
    *(volatile LAS unsigned*)(flag + F_K + kw) = 1u;
    if (kw == 0) {
      const int h = (lane >> 1) & 1;
      v2u ml[4]; float y0, y1, y2;
#pragma unroll 1
      for (unsigned i = 0; i < LDS_CAP; ++i) {
        const unsigned fl = *(volatile LAS unsigned*)(flag + F_Y + (lane & 7));
#pragma unroll
        for (int j = 0; j < 4; ++j) ml[j] = *(volatile LAS v2u*)(L + L_ML + j * 16 + h * 8);
        y0 = *(volatile LAS float*)(L + L_Y + (lane & 15) * 4); y1 = *(volatile LAS float*)(L + L_Y + 64 + (lane & 15) * 4); y2 = *(volatile LAS float*)(L + L_Y + 128 + (lane & 15) * 4);
        if (none(fl == 0u)) break;
      }
      const float M = fmx(fmx(asf(ml[0].x), asf(ml[1].x)), fmx(asf(ml[2].x), asf(ml[3].x)));
      float Ls = 0.f;
#pragma unroll
      for (int i = 0; i < 4; ++i) Ls = fmaf(__expf(asf(ml[i].x) - M), asf(ml[i].y), Ls);
      const float v = lane < 12 ? (y0 + y1) + y2 : (lane & 1) ? Ls : M;
      __builtin_amdgcn_raw_buffer_store_b64(v2u{glo(v, tg.x & 0xffu), tg.y}, rws, lane < 16 ? lane * 8 : OOB, WS_L + (g * 16 + s) * 128, AUX_SC1);
    }
    if (kw == 1) {
      pace_far(lds_get<2>(flag + F_END) + D2T - NLEAD);
      unsigned tp = lds_ld(flag + F_PUB);
#pragma unroll 1
      for (unsigned i = 0; i < SPIN_CAP && __builtin_amdgcn_readfirstlane(tp) == 0u; ++i) {
        const unsigned tn = now();
        __builtin_amdgcn_raw_buffer_store_b32(tn + NREP / 2, rws, lane < 32 ? lane * 4 : OOB, WS_N + g * 128, AUX_SC1);
#pragma unroll 1
        do tp = lds_ld(flag + F_PUB); while (__builtin_amdgcn_readfirstlane(tp) == 0u && (int)(now() - tn) < NREP);
      }
    }
    if (wg < 36) {
      int ci = L_FIN + ((lane >> 4) * 4 + (lane & 3)) * 16 + kw * 4;
      int oc = (lane & 12) == 0 ? (64 * (wg >> 2) + 32 * ((wg >> 1) & 1) + 4 * (wg & 1) + 8 * ((wg >> 2) % 3 == 2 ? (lane >> 4) : (lane >> 5) + 2 * ((lane >> 4) & 1)) + (lane & 3)) * 2 : OOB;
      unsigned tb8 = tag_byte(tg.x, lane & 3);
      int soff = lane * 16 + WS_S + wg * 16384 + kw * 4096;
      asm volatile("" : "+v"(ci), "+v"(oc), "+v"(tb8), "+v"(soff));
      const __amdgpu_buffer_rsrc_t rout = rsrc(out, 1152);
      const unsigned tend = lds_get<1>(flag + F_END);
      lds_wait<1>(flag + F_GATE);
      f32x2 gx = *(LAS f32x2*)(L + L_G + ((lane >> 4) * 4 + (lane & 3)) * 8);
      asm volatile("" : "+v"(gx));
      pace_far(tend + D2T);
      {
        const unsigned tc = lds_get<0>(flag + F_CMB) + D3P;
        unsigned tt = tend + D3T;
        tt = (int)(tc - tt) > 0 ? tc : tt;
#pragma unroll 1
        for (int i = 0; i < 64; ++i) {
          pace(tt - 5);
          const unsigned th = __builtin_amdgcn_readfirstlane(lds_ld(flag + F_HOLD));
          if (th == 0u || (int)(th - tt) <= 0) break;
          tt = th;
        }
      }
      f32x4 v;
#pragma unroll 1
      for (unsigned spin = 0; spin < SPIN_CAP; ++spin) {
        CBAR();
        v4u t[4];
#pragma unroll
        for (int i = 0; i < 4; ++i) t[i] = __builtin_amdgcn_raw_buffer_load_b128(rws, soff + i * 1024, 0, AUX_SC1);
#pragma unroll
        for (int i = 0; i < 4; ++i) t[i] ^= tb8;
        const v4u o = (t[0] | t[1]) | (t[2] | t[3]);
        v = (__builtin_bit_cast(f32x4, t[0]) + __builtin_bit_cast(f32x4, t[1])) + (__builtin_bit_cast(f32x4, t[2]) + __builtin_bit_cast(f32x4, t[3]));
        if (none((((o.x | o.y) | (o.z | o.w)) & 0xffu) != 0u)) break;
      }
      float y = sw16_sum(sw32_sum(v[0], v[1]), sw32_sum(v[2], v[3]));
      y += dpp<ROR8>(y); y += dpp<ROR4>(y);
      if (kw != 0) {
        *(volatile LAS float*)(L + ci) = y;
        *(volatile LAS unsigned*)(flag + F_F + kw) = 1u;
      } else {
        v4u fl; f32x4 f;
#pragma unroll 1
        for (unsigned i = 0; i < LDS_CAP; ++i) {
          fl = *(volatile LAS v4u*)(flag + F_F);
          f = *(volatile LAS f32x4*)(L + ci);
          if (none((fl.y & fl.z & fl.w) == 0u)) break;
        }
        const float res = gx.y + rbf(rbf((y + f[1]) + (f[2] + f[3])) * gx.x);
        __builtin_amdgcn_raw_buffer_store_b16((unsigned short)pk_bf16(res, res), rout, oc, 0, 0);
      }
    }
    __builtin_amdgcn_endpgm();
  }
  if (w >= 8) {
    ROLE();
    const int vw = w - 8, h = (lane >> 3) & 1;
    const __amdgpu_buffer_rsrc_t rb = rsrc(vc + ((unsigned)(g * 65536 + 61440 + s * 256) << 8), 65536);
    const int s0 = (vw * 4 + s * 4 + g * 2) * 1024;
    v4u d[16];
    if (s == 0) {
      typedef const __attribute__((address_space(4))) unsigned* cptr;
      const unsigned kvo = (unsigned)(g * 65536 + 61440) << 8;
      const char* p = vw == 0 ? wo + g2 * (256 * 1152) + 255 * 1152 : vw == 1 ? vc + kvo : vw == 2 ? ws + 512 : wo + g2 * (256 * 1152);
      const unsigned tch = *(cptr)p;
      asm volatile("" :: "s"(tch));
    }
    const Sel sel = make_sel(lane);
    int ooff = (lane >> 2) * 1024 + (lane & 3) * 16 + (WS_O + g * 16384 + s * 64);
    asm volatile("" : "+v"(ooff));
    const unsigned base = lds_get<2>(flag + F_BASE2), tv = base + POFF + K0X + KTAU;
    if (wg == 0 && vw == 2) {
      const __amdgpu_buffer_rsrc_t ro = rsrc(out, 1152);
      st128(ro, lane * 16, v4u{0u, 0u, 0u, 0u}); st128(ro, lane * 16 + 1024, v4u{0u, 0u, 0u, 0u});
      asm volatile("s_waitcnt vmcnt(0)" ::: "memory");
    }
    if (vw == 3) {
      pace_far(tv - SYNCAT);
      const unsigned vtouch = __builtin_amdgcn_raw_buffer_load_b32(rsrc(vc + ((unsigned)(g * 65536 + 61440) << 8), 4), 0, 0, 0);
      int dt = (int)(__builtin_amdgcn_raw_buffer_load_b32(rws, (lane & 7) * 128, WS_T, AUX_SC1) - base);
      asm volatile("" :: "v"(codew), "v"(vtouch));
      dt = (dt > 400 || dt < -400) ? 0 : dt;
      int dm = dt;
      dt += dppi<XOR1>(dt); dt += dppi<XOR2>(dt); dt += dppi<HMIR>(dt);
      dm = max(dm, dppi<XOR1>(dm)); dm = max(dm, dppi<XOR2>(dm)); dm = max(dm, dppi<HMIR>(dm));
      lds_st(flag + F_END, (tv + max(VTAU + (dt >> 3), VTAU - 25 + dm)) | 1u);
    }
    unsigned tw = tv;
    int qd = VTAU / 4;
    if (vw) {
      pace_far(tv + VTAU / 4 + (vw - 1) * (VTAU * 3 / 16) - 8);
      const unsigned te = __builtin_amdgcn_readfirstlane(lds_ld(flag + F_END));
      if (te) qd = max(((int)(te - tv) - VTAU / 4) / 3, VTAU * 3 / 16);
      tw = tv + VTAU / 4 + (vw - 1) * qd;
    }
    pace_far(tw - 4);
#pragma unroll
    for (int i = 0; i < 16; ++i) {
      if (i % 2 == 0) pace(tw + ((i * qd) >> 4));
      d[i] = __builtin_amdgcn_raw_buffer_load_b128(rb, lane * 16, (s0 + piece(i) * 1024) & 0xffff, AUX_NT); SCHED();
    }
    lds_wait<1>(flag + F_K + (lane & 3));
    v4u pr[4];
#pragma unroll
    for (int i = 0; i < 4; ++i) pr[i] = *(LAS v4u*)(L + L_P + vw * 512 + (lane >> 3) * 64 + i * 16);
    float mm[4];
#pragma unroll
    for (int i = 0; i < 4; ++i) mm[i] = *(LAS float*)(L + L_ML + i * 16 + h * 8);
    const float mine = *(LAS float*)(L + L_ML + vw * 16 + h * 8);
    unsigned tb8 = *(LAS unsigned char*)(L + L_TAG + (lane & 3));
    float f = __expf(mine - fmx(fmx(mm[0], mm[1]), fmx(mm[2], mm[3])));
    asm volatile("" : "+v"(f), "+v"(tb8));
    f32x4 acc = {0.f, 0.f, 0.f, 0.f};
#pragma unroll
    for (int i = 0; i < 12; ++i) { const int idx = (i & 1) * 8 + (i >> 1); acc = mfma(d[i], onehot(sel, pr[idx >> 2][idx & 3]), acc); }
    v4u b12 = onehot(sel, pr[1][2]), b13 = onehot(sel, pr[3][2]), b14 = onehot(sel, pr[1][3]), b15 = onehot(sel, pr[3][3]);
    asm volatile("" : "+v"(b12), "+v"(b13), "+v"(b14), "+v"(b15));
    if (vw != 3) {
      acc = mfma(d[12], b12, acc); acc = mfma(d[13], b13, acc); acc = mfma(d[14], b14, acc); acc = mfma(d[15], b15, acc);
      *(volatile LAS f32x4*)(L + L_RED + vw * 1024 + lane * 16) = acc * f;
      *(volatile LAS unsigned*)(flag + F_V + vw) = 1u;
    } else {
      v4u fl; f32x4 r0, r1, r2;
#pragma unroll 1
      for (unsigned i = 0; i < LDS_CAP; ++i) {
        fl = *(volatile LAS v4u*)(flag + F_V);
        r0 = *(volatile LAS f32x4*)(L + L_RED + lane * 16);
        r1 = *(volatile LAS f32x4*)(L + L_RED + 1024 + lane * 16);
        r2 = *(volatile LAS f32x4*)(L + L_RED + 2048 + lane * 16);
        if (none((fl.x & fl.y & fl.z) == 0u)) break;
      }
      f32x4 pre = (r0 + r1) + r2;
      asm volatile("" : "+v"(pre));
      SCHED();
      acc = mfma(d[12], b12, acc); acc = mfma(d[13], b13, acc); acc = mfma(d[14], b14, acc); acc = mfma(d[15], b15, acc);
      const f32x4 o = __builtin_elementwise_fma(acc, f32x4{f, f, f, f}, pre);
      st128(rws, ooff, v4u{glo(o[0], tb8), glo(o[1], tb8), glo(o[2], tb8), glo(o[3], tb8)});
      SCHED();
      lds_st(flag + F_PUB, now() | 1u);
    }
    __builtin_amdgcn_endpgm();
  }
  if (w != 0) {
    ROLE();
    const __amdgpu_buffer_rsrc_t rx = rsrc(x, 1152);
    const int ow = w - 1, cg = (g >> 3) * 32 + (g & 1) * 16 + s, fq = (g >> 1) & 3;
    const v4u xa = __builtin_amdgcn_raw_buffer_load_b128(rx, lane * 16, 0, 0);
    const unsigned xb = __builtin_amdgcn_raw_buffer_load_b16(rx, lane * 2 + 1024, 0, 0);
    const unsigned e3 = __builtin_amdgcn_raw_buffer_load_b16(rx, min(lane, 8) * 2, cg * 18, 0);
    const int foff = fq * 2304 + ow * 768;
    const v3u w1v = __builtin_amdgcn_raw_buffer_load_b96(rsrc(w1, 9216), lane * 12, foff, 0);
    const v3u b1v = __builtin_amdgcn_raw_buffer_load_b96(rsrc(b1, 9216), lane * 12, foff, 0);
    const v3u w2v = __builtin_amdgcn_raw_buffer_load_b96(rsrc(w2, 9216), lane * 12, foff, 0);
    const unsigned b2v = __builtin_amdgcn_raw_buffer_load_b16(rsrc(b2, 2), 0, 0, 0);
    const __amdgpu_buffer_rsrc_t rwo = rsrc(wo + (unsigned)((g2 * 256 + ((s >> 1) & 1) * 128 + 32 * (s >> 2) + 4 * (s & 1)) * 1152 + ow * 384), 28 * 1152 - ow * 384);
    const int wvo = (16 * (z & 1) + (z >> 1)) * 1152 + (lane & 7) * 16;
    SCHED();
    f32x2 fa[4];
#pragma unroll
    for (int i = 0; i < 4; ++i) fa[i] = bf2(xa[i]);
    float m, rden;
    ln_stats(fa, bflo(xb), m, rden);
    const f32x2 hv = bf2(ln_apply2(f32x2{bflo(e3), bflo(xb)}, m, rden));
#pragma unroll
    for (int i = 0; i < 4; ++i) fa[i] = bf2(ln_apply2(fa[i], m, rden));
    ln_stats(fa, hv.y, m, rden);
    const float hl = bflo(ln_apply2(hv, m, rden));
    f32x2 w1f[3], b1f[3];
#pragma unroll
    for (int j = 0; j < 3; ++j) { w1f[j] = bf2(w1v[j]); b1f[j] = bf2(b1v[j]); }
    constexpr float C1 = -2.0f * 0.7978845608028654f * 1.4426950408889634f, C2 = C1 * 0.044715f;
    float ysum = 0.f;
#pragma unroll 1
    for (int c = 0; c < 9; ++c) {
      const float hc = asf(__builtin_amdgcn_readlane(asu(hl), c));
      float y = 0.f;
#pragma unroll
      for (int j = 0; j < 3; ++j) {
        const f32x2 u = bf2(pk2(bf2(pk2(w1f[j] * hc)) + b1f[j]));
        const f32x2 t = u * __builtin_elementwise_fma(u * u, f32x2{C2, C2}, f32x2{C1, C1});
        const f32x2 dn = f32x2{__builtin_amdgcn_exp2f(t.x), __builtin_amdgcn_exp2f(t.y)} + 1.0f;
        const unsigned gl = pk2(u * f32x2{__builtin_amdgcn_rcpf(dn.x), __builtin_amdgcn_rcpf(dn.y)});
        y = __builtin_amdgcn_fdot2_f32_bf16(asb(gl), asb(w2v[j]), y, false);
      }
      y = row_sum(y);
      if ((lane & 15) == c) ysum = y;
    }
    ysum = sw16_sum(ysum, ysum); ysum = sw32_sum(ysum, ysum);
    *(LAS float*)(L + L_Y + ow * 64 + (lane & 15) * 4) = ysum;
    *(LAS float*)(L + L_B2) = bflo(b2v);
    lds_set(flag + F_Y + ow);
    const unsigned wtouch = __builtin_amdgcn_raw_buffer_load_b32(rsrc(wo + g2 * (256 * 1152), 256 * 1152), ow == 0 ? (lane & 1) * (255 * 1152) : OOB, 0, 0);
    lds_wait<1>(flag + F_TAG);
    const int sb = ((ow * 12 + ((lane >> 4) & 1) * 2 + ((lane >> 2) & 1)) * 16384 + (lane & 3) * 16 + WS_S + wg * 64) | ((lane & 8) ? OOB : 0);
    int soff = sb + (lane >> 5) * 65536, soff2 = sb + 131072 + (lane >> 5) * 8;
    const v2u tg = *(LAS v2u*)(L + L_TAG);
    unsigned tbs = tag_byte(tg.x, lane & 3), tba = tag_byte(tg.x, lane >> 4);
    int wv1 = wvo + 8 * 1152;
    int aoff = (lane & 7) * 64 + z * 8 + WS_O + g2 * 16384 + s * 1024;
    asm volatile("" : "+v"(soff), "+v"(soff2), "+v"(tbs), "+v"(wv1), "+v"(tba), "+v"(aoff) : "v"(wtouch));
    const Sel sel = make_sel(lane);
    const unsigned tend = lds_get<1>(flag + F_END);
    unsigned tw = tend + WOFF;
    pace_far(tw);
    tw += ow * WTAU / 3;
    pace(tw);
    v4u d[6];
#pragma unroll
    for (int j = 0; j < 6; ++j) {
      if (j) pace(tw + j * WTAU);
      d[j] = __builtin_amdgcn_raw_buffer_load_b128(rwo, ((j & 1) ? wv1 : wvo) + (j >> 1) * 128, 0, AUX_NT);
      SCHED();
    }
    {
      const unsigned tc = lds_get<0>(flag + F_PUB) + D2P, tt = tend + D2T, tr = (int)(tc - tt) > 0 ? tc : tt;
      pace(tr);
    }
    CBAR();
    v2u a0 = __builtin_amdgcn_raw_buffer_load_b64(rws, aoff, 0, AUX_SC1);
    v2u a1 = __builtin_amdgcn_raw_buffer_load_b64(rws, aoff + 512, 0, AUX_SC1);
    SCHED();
    lds_wait<0>(flag + F_W);
    const f32x2 wt = *(LAS f32x2*)(L + L_W + (lane & 7) * 8);
    float w0 = wt.x, w1s = wt.y;
    asm volatile("" : "+v"(w0), "+v"(w1s));
    SCHED();
    a0 ^= tba; a1 ^= tba;
    if (__builtin_expect(!none((((a0.x | a0.y) | (a1.x | a1.y)) & 0xffu) != 0u), 0)) {
#pragma unroll 1
      for (unsigned spin = 0; spin < SPIN_CAP; ++spin) {
        CBAR();
        a0 = __builtin_amdgcn_raw_buffer_load_b64(rws, aoff, 0, AUX_SC1);
        a1 = __builtin_amdgcn_raw_buffer_load_b64(rws, aoff + 512, 0, AUX_SC1);
        a0 ^= tba; a1 ^= tba;
        if (none((((a0.x | a0.y) | (a1.x | a1.y)) & 0xffu) != 0u)) break;
      }
    }
    const f32x2 ov = __builtin_elementwise_fma(__builtin_bit_cast(f32x2, a1), f32x2{w1s, w1s}, __builtin_bit_cast(f32x2, a0) * w0);
    float o0 = ov.x, o1 = ov.y;
    asm volatile("" : "+v"(o0), "+v"(o1));
    o0 += dpp<XOR1>(o0); o1 += dpp<XOR1>(o1); asm volatile("" : "+v"(o0), "+v"(o1));
    o0 += dpp<XOR2>(o0); o1 += dpp<XOR2>(o1); asm volatile("" : "+v"(o0), "+v"(o1));
    o0 += dpp<HMIR>(o0); o1 += dpp<HMIR>(o1);
    const v4u b0 = onehot(sel, pk_bf16(o0, o0)), b1o = onehot(sel, pk_bf16(o1, o1));
    f32x4 ac[3];
#pragma unroll
    for (int j = 0; j < 3; ++j) { ac[j] = mfma(d[2 * j], b0, f32x4{0.f, 0.f, 0.f, 0.f}); }
#pragma unroll
    for (int j = 0; j < 3; ++j) { ac[j] = mfma(d[2 * j + 1], b1o, ac[j]); }
    v4u res;
#pragma unroll
    for (int r = 0; r < 4; ++r) {
      auto ab = __builtin_amdgcn_permlane32_swap(asu(ac[0][r]), asu(ac[1][r]), false, false);
      float ya = asf(ab[0]) + dpp<ROR8>(asf(ab[1]));
      asm("" : "+v"(ya));
      res[r] = glo(ya, tbs);
    }
    st128(rws, soff, res);
    {
      auto c01 = __builtin_amdgcn_permlane32_swap(asu(ac[2][0]), asu(ac[2][1]), false, false);
      auto c23 = __builtin_amdgcn_permlane32_swap(asu(ac[2][2]), asu(ac[2][3]), false, false);
      float y0 = asf(c01[0]) + dpp<ROR8>(asf(c01[1])), y1 = asf(c23[0]) + dpp<ROR8>(asf(c23[1]));
      asm("" : "+v"(y0)); asm("" : "+v"(y1));
      __builtin_amdgcn_raw_buffer_store_b64(v2u{glo(y0, tbs), glo(y1, tbs)}, rws, soff2, 0, AUX_SC1);
    }
    SCHED();
    if (ow == 0) lds_st(flag + F_CMB, now() | 1u);
    __builtin_amdgcn_endpgm();
  }
  {
    const int cq = s & 3, rq = s >> 2;
    int wvoff = z * (18 * 8192) + (lane & 7) * 16, wbase = rq * 144 * 8192 + g * 512 + cq * 128;
    asm volatile("" : "+v"(wvoff));
    asm volatile("" : "+s"(wbase));
    ROLE();
    const __amdgpu_buffer_rsrc_t rb = rsrc(wq + (unsigned)wbase, 144 * 8192);
    const __amdgpu_buffer_rsrc_t rx = rsrc(x, 1152);
    v4u d[18];
#pragma unroll
    for (int i = 0; i < 2; ++i) { d[i] = __builtin_amdgcn_raw_buffer_load_b128(rb, wvoff, i * 8192, AUX_NT); if (i & 1) SCHED(); }
    SCHED();
    const v4u xa = __builtin_amdgcn_raw_buffer_load_b128(rx, lane * 16, 0, 0);
    const unsigned xb = __builtin_amdgcn_raw_buffer_load_b16(rx, lane * 2 + 1024, 0, 0);
    const v4u e0 = __builtin_amdgcn_raw_buffer_load_b128(rx, z * 36, rq * 288, 0);
    const v4u e1 = __builtin_amdgcn_raw_buffer_load_b128(rx, z * 36 + 16, rq * 288, 0);
    const unsigned e3 = __builtin_amdgcn_raw_buffer_load_b32(rx, z * 36 + 32, rq * 288, 0);
    SCHED();
#pragma unroll
    for (int i = 2; i < 18; ++i) { d[i] = __builtin_amdgcn_raw_buffer_load_b128(rb, wvoff, i * 8192, AUX_NT); if (i & 1) SCHED(); }
    SCHED();
    const int late = (int)(now() - t0) - XLAT;
    const unsigned base = (t0 + (late > XTOL ? late + min(late - XTOL, XADD) : 0)) | 1u;
    lds_st(flag + F_BASE, base);
    const v2u e2 = __builtin_amdgcn_raw_buffer_load_b64(rws, 0, WS_E, 0);
    SCHED();
    const int qoff = (lane & 8) ? OOB : (cq * 32 + (lane >> 5) * 16 + ((lane >> 4) & 1) * 8 + (lane & 7)) * 8;
    f32x2 fa[4];
#pragma unroll
    for (int i = 0; i < 4; ++i) fa[i] = bf2(xa[i]);
    float m, rden;
    ln_stats(fa, bflo(xb), m, rden);
    const Sel sel = make_sel(lane);
    const unsigned xr[9] = {e0.x, e0.y, e0.z, e0.w, e1.x, e1.y, e1.z, e1.w, e3};
    unsigned xn[9];
#pragma unroll
    for (int i = 0; i < 9; ++i) xn[i] = ln_apply2(bf2(xr[i]), m, rden);
    f32x4 acc = {0.f, 0.f, 0.f, 0.f};
#pragma unroll
    for (int i = 0; i < 18; ++i) acc = mfma(d[i], onehot(sel, (i & 1) ? duphi(xn[i >> 1]) : duplo(xn[i >> 1])), acc);
    SCHED();
    const unsigned long long e64 = (((unsigned long long)(unsigned)__builtin_amdgcn_readfirstlane(e2.y) << 32) | (unsigned)__builtin_amdgcn_readfirstlane(e2.x)) + 0x1111111111111111ull;
    const unsigned tlo = (unsigned)e64, thi = (unsigned)(e64 >> 32);
    const unsigned tn = (unsigned)(e64 >> (8 * (lane & 7))) & 0xfu;
    {
      auto r01 = __builtin_amdgcn_permlane32_swap(asu(acc[0]), asu(acc[1]), false, false);
      auto r23 = __builtin_amdgcn_permlane32_swap(asu(acc[2]), asu(acc[3]), false, false);
      const float y0 = asf(r01[0]) + dpp<ROR8>(asf(r01[1])), y1 = asf(r23[0]) + dpp<ROR8>(asf(r23[1]));
      __builtin_amdgcn_raw_buffer_store_b64(v2u{(asu(y0) & ~0xfu) | tn, (asu(y1) & ~0xfu) | tn}, rws, qoff, WS_Q + g * 4096 + rq * 1024, AUX_SC1);
    }
    *(LAS v2u*)(L + L_TAG) = v2u{tlo, thi};
    lds_set(flag + F_TAG);
    {
      const unsigned tp = (unsigned)(e64 >> (16 * (lane & 3)));
      unsigned tn0 = tp & 0xfu, tn1 = (tp >> 8) & 0xfu;
      int c0 = L_Q + (64 * (lane >> 4) + 32 * ((lane >> 2) & 1) + 8 * ((lane >> 3) & 1) + 2 * (lane & 3)) * 2;
      asm volatile("" : "+v"(tn0), "+v"(tn1), "+v"(c0));
      const unsigned tq = now() + 40;
      pace_far(lds_get<1>(flag + F_BASE2) + QAT); pace_far(tq);
      v4u q[4];
#pragma unroll 1
      for (unsigned spin = 0; spin < SPIN_CAP; ++spin) {
        CBAR();
#pragma unroll
        for (int i = 0; i < 4; ++i) q[i] = __builtin_amdgcn_raw_buffer_load_b128(rws, lane * 16 + i * 1024, WS_Q + g * 4096, AUX_SC1);
#pragma unroll
        for (int i = 0; i < 4; ++i) q[i] ^= v4u{tn0, tn0, tn1, tn1};
        const v4u o = (q[0] | q[1]) | (q[2] | q[3]);
        if (none((((o.x | o.y) | (o.z | o.w)) & 0xfu) != 0u)) break;
      }
      const f32x4 v = (__builtin_bit_cast(f32x4, q[0]) + __builtin_bit_cast(f32x4, q[1])) + (__builtin_bit_cast(f32x4, q[2]) + __builtin_bit_cast(f32x4, q[3]));
      *(volatile LAS unsigned*)(L + c0) = pk_bf16(v[0], v[2]);
      *(volatile LAS unsigned*)(L + c0 + 32) = pk_bf16(v[1], v[3]);
      *(volatile LAS unsigned*)(flag + F_Q) = 1u;
    }
    const int slot = lane >> 2;
    const int col = 64 * (wg >> 2) + 32 * ((wg >> 1) & 1) + 4 * (wg & 1) + 8 * ((wg >> 2) % 3 == 2 ? (slot >> 2) : (slot >> 3) + 2 * ((slot >> 2) & 1)) + (slot & 3);
    const int cg = col / 9;
    const int yoff = wg < 36 ? WS_L + ((cg >> 5) * 128 + (lane & 3) * 32 + (cg & 31)) * 128 + (col - cg * 9) * 8 : OOB;
    const int moff = (lane & 7) * 128 + WS_L + g2 * 2048 + 96 + ((s >> 1) & 1) * 16;
    const float xo = bflo(__builtin_amdgcn_raw_buffer_load_b16(rx, col * 2, 0, 0));
    const unsigned nw = __builtin_amdgcn_raw_buffer_load_b32(rws, wg < 36 ? (lane & 15) * 128 : OOB, WS_N, AUX_SC1);
    const unsigned tend = lds_get<2>(flag + F_END);
    pace_far(tend + MLT);
    v2u y = __builtin_amdgcn_raw_buffer_load_b64(rws, yoff, 0, AUX_SC1);
    {
      v4u ml0, ml1;
#pragma unroll 1
      for (unsigned spin = 0; spin < SPIN_CAP; ++spin) {
        CBAR();
        ml0 = __builtin_amdgcn_raw_buffer_load_b128(rws, moff, 0, AUX_SC1);
        ml1 = __builtin_amdgcn_raw_buffer_load_b128(rws, moff + 1024, 0, AUX_SC1);
        if (none((gbad(ml0, tlo & 0xffu, thi) | gbad(ml1, tlo & 0xffu, thi)) != 0u)) break;
      }
      float M = fmx(gval(ml0.x), gval(ml1.x));
      M = fmx(M, dpp<XOR1>(M)); M = fmx(M, dpp<XOR2>(M)); M = fmx(M, dpp<HMIR>(M));
      const float w0 = __expf(gval(ml0.x) - M), w1s = __expf(gval(ml1.x) - M);
      float Ls = w0 * gval(ml0.z) + w1s * gval(ml1.z);
      Ls += dpp<XOR1>(Ls); Ls += dpp<XOR2>(Ls); Ls += dpp<HMIR>(Ls);
      const float rl = __builtin_amdgcn_rcpf(Ls);
      *(LAS f32x2*)(L + L_W + (lane & 7) * 8) = f32x2{w0 * rl, w1s * rl};
      lds_set(flag + F_W);
    }
    if (wg == 255) {
      pace_far(tend + D2T + 15);
#pragma unroll 1
      for (unsigned spin = 0; spin < SPIN_CAP; ++spin) {
        CBAR();
        unsigned bad = 0;
#pragma unroll
        for (int i = 0; i < 4; ++i) { const v2u t = __builtin_amdgcn_raw_buffer_load_b64(rws, lane * 128, WS_L + 96 + i * 8192, AUX_SC1); bad |= ((t.x ^ tlo) & 0xffu) | (t.y ^ thi); }
        if (none(bad != 0u)) break;
      }
      __builtin_amdgcn_raw_buffer_store_b64(v2u{tlo, thi}, rws, lane == 0 ? WS_E : OOB, 0, AUX_SC1);
    }
    if (wg < 36) {
      lds_wait<1>(flag + F_Y);
      const float bb = *(LAS float*)(L + L_B2);
#pragma unroll 1
      for (unsigned spin = 0; spin < SPIN_CAP; ++spin) {
        if (none((((y.x ^ tlo) & 0xffu) | (y.y ^ thi)) != 0u)) break;
        CBAR();
        y = __builtin_amdgcn_raw_buffer_load_b64(rws, yoff, 0, AUX_SC1);
      }
      float sy = gval(y.x);
      sy += dpp<XOR1>(sy); sy += dpp<XOR2>(sy);
      const float yy = rbf(rbf(sy) + bb);
      const float gate = rbf(yy * __builtin_amdgcn_rcpf(1.0f + __expf(-yy)));
      *(LAS f32x2*)(L + L_G + slot * 8) = f32x2{gate, xo};
      lds_set(flag + F_GATE);
      {
        asm volatile("" :: "v"(nw));
        unsigned tt = tend + D3T;
#pragma unroll 1
        for (int i = 0; i < 64; ++i) {
          pace(tt - NCHK);
          CBAR();
          int dn = (int)(__builtin_amdgcn_raw_buffer_load_b32(rws, (lane & 15) * 128, WS_N, AUX_SC1) + NADD - tt);
          dn = dn > 200 ? 0 : dn;
          dn = max(dn, dppi<ROR8>(dn)); dn = max(dn, dppi<ROR4>(dn)); dn = max(dn, dppi<ROR2>(dn)); dn = max(dn, dppi<ROR1>(dn));
          dn = __builtin_amdgcn_readfirstlane(dn);
          if (dn <= 0) break;
          tt += dn;
          lds_st(flag + F_HOLD, tt | 1u);
        }
      }
    }
  }
}
}

extern "C" void kernel_launch(void* const* d_in, const int* in_sizes, int n_in, void* d_out, int out_size, void* d_ws, size_t ws_size, hipStream_t stream) {
  fused_decode<<<NWG, NTHR, 0, stream>>>((const char*)d_in[3], (const char*)d_in[0], (const char*)d_in[1], (char*)d_ws, (const char*)d_in[2],
      (const char*)d_in[4], (char*)d_out, (const char*)d_in[6], (const char*)d_in[7], (const char*)d_in[8], (const char*)d_in[9]);
}
```
